```python
import torch

def _softmax(x, axis=-1):
    return torch.softmax(x, dim=axis)
def _gelu(x):
    return torch.nn.functional.gelu(x, approximate="tanh")
def _silu(x):
    return torch.nn.functional.silu(x)
def _sigmoid(x):
    return torch.sigmoid(x)
def _layernorm(x, axis=-1, eps=1e-5):
    m = x.mean(dim=axis, keepdim=True)
    v = x.var(dim=axis, keepdim=True, unbiased=False)
    return (x - m) / torch.sqrt(v + eps)
def _scatter_add(tgt, idx, src, axis=0):
    return tgt.clone().index_add_(axis, idx, src)
def _segment_sum(src, seg, n):
    out = torch.zeros((n,) + src.shape[1:], dtype=src.dtype)
    return out.index_add_(0, seg, src)
def _pad(x, pw):
    flat = tuple(v for pair in reversed(pw) for v in pair)
    return torch.nn.functional.pad(x, flat)

kernel_name = 'standalone__controlnet__diffusion'

def setup_inputs():

    _w = (lambda a: (lambda t: t.to(torch.bfloat16) if t.is_floating_point() else t)(torch.as_tensor(a)))
    B, HW, C = 2, 128, 128
    sc = C ** -0.5
    return {
        "x_frozen": _w((lambda *s: torch.randn(*s, dtype=torch.bfloat16) * 0.05)(B, HW, HW, C)),
        "x": _w((lambda *s: torch.randn(*s, dtype=torch.bfloat16) * 0.1)(B, HW, HW, C)),
        "c": _w((lambda *s: torch.randn(*s, dtype=torch.bfloat16) * 0.4)(B, HW, HW, C)),
        "w_z1": _w((lambda *s: torch.randn(*s, dtype=torch.bfloat16) * 0.4)(C, C) * sc),
        "w_z2": _w((lambda *s: torch.randn(*s, dtype=torch.bfloat16) * 0.1)(C, C) * sc),
        "w_conv": _w((lambda *s: torch.randn(*s, dtype=torch.bfloat16) * 0.1)(3, 3, C, C) * sc / 3.0),
    }

def reference(x_frozen, x, c, w_z1, w_z2, w_conv):
    B, H, W, C = x.shape
    h_in = x + torch.einsum("bhwc,cd->bhwd", c, w_z1)
    h = _silu(_layernorm(h_in, axis=-1))
    hp = _pad(h, ((0, 0), (1, 1), (1, 1), (0, 0)))
    acc = h * 0.0
    for di in range(3):
        for dj in range(3):
            acc = acc + torch.einsum("bhwc,cd->bhwd", hp[:, di:di + H, dj:dj + W, :], w_conv[di, dj])
    h = h_in + acc
    return x_frozen + torch.einsum("bhwc,cd->bhwd", h, w_z2)
```

```cpp
#include <hip/hip_runtime.h>
#include <hip/hip_bf16.h>
#include <stdint.h>

typedef unsigned short u16;
__device__ __forceinline__ float bf2f(u16 v) { return __uint_as_float(((unsigned)v) << 16); }
__device__ __forceinline__ u16 f2bf(float f) { unsigned u = __float_as_uint(f); u += 0x7fffu + ((u >> 16) & 1u); return (u16)(u >> 16); }

__global__ void k1(const u16* x, const u16* c, const u16* wz1, float* hin, float* h) {
    int p = blockIdx.x, n = threadIdx.x;
    __shared__ float sc[128], red[128];
    sc[n] = bf2f(c[p * 128 + n]);
    __syncthreads();
    float acc = 0.f;
    for (int k = 0; k < 128; ++k) acc += sc[k] * bf2f(wz1[k * 128 + n]);
    float v = acc + bf2f(x[p * 128 + n]);
    red[n] = v; __syncthreads();
    float s = 0.f; for (int k = 0; k < 128; ++k) s += red[k];
    float m = s / 128.f;
    float q = 0.f; for (int k = 0; k < 128; ++k) { float d = red[k] - m; q += d * d; }
    float var = q / 128.f;
    float y = (v - m) / sqrtf(var + 1e-5f);
    float sl = y / (1.f + expf(-y));
    hin[p * 128 + n] = v;
    h[p * 128 + n] = sl;
}
__global__ void k2(const u16* xf, const float* hin, const float* h, const u16* wz2, const u16* wconv, u16* out) {
    int p = blockIdx.x, n = threadIdx.x;
    int b = p >> 14, y = (p >> 7) & 127, xx = p & 127;
    __shared__ float sh[128], hh[128];
    float acc = 0.f;
    for (int di = 0; di < 3; ++di) for (int dj = 0; dj < 3; ++dj) {
        int yy = y + di - 1, xc = xx + dj - 1;
        bool ok = yy >= 0 && yy < 128 && xc >= 0 && xc < 128;
        __syncthreads();
        sh[n] = ok ? h[((b << 14) + (yy << 7) + xc) * 128 + n] : 0.f;
        __syncthreads();
        for (int k = 0; k < 128; ++k) acc += sh[k] * bf2f(wconv[((di * 3 + dj) * 128 + k) * 128 + n]);
    }
    hh[n] = hin[p * 128 + n] + acc;
    __syncthreads();
    float o = 0.f;
    for (int k = 0; k < 128; ++k) o += hh[k] * bf2f(wz2[k * 128 + n]);
    out[p * 128 + n] = f2bf(o + bf2f(xf[p * 128 + n]));
}

extern "C" void kernel_launch(void* const* d_in, const int* in_sizes, int n_in,
                              void* d_out, int out_size, void* d_ws, size_t ws_size,
                              hipStream_t stream) {
    const u16* xf = (const u16*)d_in[0];
    const u16* x = (const u16*)d_in[1];
    const u16* c = (const u16*)d_in[2];
    const u16* wz1 = (const u16*)d_in[3];
    const u16* wz2 = (const u16*)d_in[4];
    const u16* wconv = (const u16*)d_in[5];
    float* hin = (float*)d_ws;
    float* h = hin + 32768 * 128;
    k1<<<32768, 128, 0, stream>>>(x, c, wz1, hin, h);
    k2<<<32768, 128, 0, stream>>>(xf, hin, h, wz2, wconv, (u16*)d_out);
}
```

```cpp
#include <hip/hip_runtime.h>
#include <stdint.h>

typedef unsigned short u16;
typedef short s16x4 __attribute__((ext_vector_type(4)));
typedef short s16x8 __attribute__((ext_vector_type(8)));
typedef __bf16 bf16x8 __attribute__((ext_vector_type(8)));
typedef __bf16 bf16x2 __attribute__((ext_vector_type(2)));
typedef float f32x2 __attribute__((ext_vector_type(2)));
typedef float f32x4 __attribute__((ext_vector_type(4)));
typedef float f32x16 __attribute__((ext_vector_type(16)));
typedef unsigned u32x4 __attribute__((ext_vector_type(4)));
#define DI __device__ __forceinline__
#define LDSP(T) __attribute__((address_space(3))) T*

constexpr int H_RS = 2944, H_PS = 272;
constexpr int L_H = 0;
constexpr int L_C = 52992;
constexpr int L_W = L_C + 49152;
constexpr int L_ST = L_W + 32768;
constexpr int L_TOTAL = L_ST + 2048;

DI unsigned cvtpk(float lo, float hi) { f32x2 v = {lo, hi}; bf16x2 b = __builtin_convertvector(v, bf16x2); return __builtin_bit_cast(unsigned, b); }
DI float bflo(unsigned u) { return __uint_as_float(u << 16); }
DI float bfhi(unsigned u) { return __uint_as_float(u & 0xffff0000u); }
DI float swap_add(float v) {
  auto r = __builtin_amdgcn_permlane32_swap(__float_as_uint(v), __float_as_uint(v), false, false);
  return __uint_as_float(r[0]) + __uint_as_float(r[1]);
}
DI void glds16(const void* g, char* lds_wave_base) {
  __builtin_amdgcn_global_load_lds((const __attribute__((address_space(1))) unsigned*)g, (LDSP(unsigned))lds_wave_base, 16, 0, 0);
}
DI bf16x8 tr_frag(const char* lds, int off) {
  s16x4 lo = __builtin_amdgcn_ds_read_tr16_b64_v4i16((LDSP(s16x4))(lds + off));
  s16x4 hi = __builtin_amdgcn_ds_read_tr16_b64_v4i16((LDSP(s16x4))(lds + off + 1024));
  return __builtin_bit_cast(bf16x8, __builtin_shufflevector(lo, hi, 0, 1, 2, 3, 4, 5, 6, 7));
}
#define MFMA(a, b, c) __builtin_amdgcn_mfma_f32_32x32x16_bf16((a), (b), (c), 0, 0, 0)

DI void stage_w(const u16* w, char* dst, int wid, int lane) {
  const int chunk = (lane & 15) ^ (4 * (lane >> 4));
  const char* src = (const char*)w + (lane >> 4) * 256 + chunk * 16;
  #pragma unroll
  for (int i = 0; i < 8; ++i) {
    int piece = wid * 8 + i;
    glds16(src + piece * 1024, dst + piece * 1024);
  }
}

__global__ __launch_bounds__(256, 1) void cn_kernel(const u16* __restrict__ xf, const u16* __restrict__ x, const u16* __restrict__ c,
                                                    const u16* __restrict__ wz1, const u16* __restrict__ wz2, const u16* __restrict__ wconv,
                                                    u16* __restrict__ out) {
  extern __shared__ __attribute__((aligned(16))) char lds[];
  const int tid = threadIdx.x, lane = tid & 63, wid = __builtin_amdgcn_readfirstlane(tid >> 6);
  const int r = lane & 31, hh = lane >> 5;
  const int blk = blockIdx.x, xcd = blk & 7, idx = blk >> 3;
  const int b = xcd >> 2, y0 = (xcd & 3) * 32 + (idx >> 4) * 16, x0 = (idx & 15) * 8;
  const size_t img = (size_t)b * 128 * 128;

  stage_w(wz1, lds + L_W, wid, lane);
  #pragma unroll
  for (int i = 0; i < 12; ++i) {
    int piece = wid * 12 + i;
    int p = piece * 4 + (lane >> 4);
    int pp = p < 180 ? p : 179;
    int row = pp / 10, col = pp - row * 10;
    int gy = min(max(y0 - 1 + row, 0), 127), gx = min(max(x0 - 1 + col, 0), 127);
    int chunk = (lane & 15) ^ (p & 15);
    glds16((const char*)c + (img + gy * 128 + gx) * 256 + chunk * 16, lds + L_C + piece * 1024);
  }
  u32x4 xr[6][2];
  bool valid[6];
  int haddr[6];
  #pragma unroll
  for (int j = 0; j < 6; ++j) {
    int p = j * 32 + r;
    int pp = p < 180 ? p : 179;
    int row = pp / 10, col = pp - row * 10;
    int gy = y0 - 1 + row, gx = x0 - 1 + col;
    valid[j] = gy >= 0 && gy < 128 && gx >= 0 && gx < 128;
    haddr[j] = p < 180 ? row * H_RS + col * H_PS : -1;
    gy = min(max(gy, 0), 127); gx = min(max(gx, 0), 127);
    const char* src = (const char*)x + (img + gy * 128 + gx) * 256 + wid * 64 + hh * 32;
    xr[j][0] = *(const u32x4*)src;
    xr[j][1] = *(const u32x4*)(src + 16);
  }
  __syncthreads();
  const int q = (lane & 15) >> 2, p4 = lane & 3, blkb = (lane >> 4) & 1;
  const int trbase = (8 * hh + q) * 256 + 64 * (wid ^ q) + 32 * (p4 & 1) + 16 * blkb + 8 * (p4 >> 1);
  bf16x8 A[8];
  #pragma unroll
  for (int s = 0; s < 8; ++s) A[s] = tr_frag(lds + L_W, trbase + s * 4096);
  __syncthreads();
  stage_w(wconv, lds + L_W, wid, lane);

  #pragma unroll
  for (int j = 0; j < 6; ++j) {
    const int p = j * 32 + r;
    f32x16 acc;
    #pragma unroll
    for (int e = 0; e < 4; ++e) {
      acc[2 * e] = bflo(xr[j][0][e]); acc[2 * e + 1] = bfhi(xr[j][0][e]);
      acc[8 + 2 * e] = bflo(xr[j][1][e]); acc[8 + 2 * e + 1] = bfhi(xr[j][1][e]);
    }
    #pragma unroll
    for (int s = 0; s < 8; ++s) {
      bf16x8 B = *(const bf16x8*)(lds + L_C + p * 256 + 16 * ((2 * s + hh) ^ (p & 15)));
      acc = MFMA(A[s], B, acc);
    }
    float sum = 0.f;
    #pragma unroll
    for (int i = 0; i < 16; ++i) sum += acc[i];
    sum = swap_add(sum);
    float mw = sum * (1.f / 32.f);
    float qq = 0.f;
    #pragma unroll
    for (int i = 0; i < 16; ++i) { float d = acc[i] - mw; qq += d * d; }
    qq = swap_add(qq);
    char* st = lds + L_ST + (j & 1) * 1024;
    if (hh == 0) *(f32x2*)(st + r * 32 + wid * 8) = f32x2{mw, qq};
    asm volatile("s_waitcnt lgkmcnt(0)" ::: "memory");
    __builtin_amdgcn_s_barrier();
    f32x4 s0 = *(const f32x4*)(st + r * 32), s1 = *(const f32x4*)(st + r * 32 + 16);
    float mean = (s0[0] + s0[2] + s1[0] + s1[2]) * 0.25f;
    float d0 = s0[0] - mean, d1 = s0[2] - mean, d2 = s1[0] - mean, d3 = s1[2] - mean;
    float M2 = (s0[1] + s0[3] + s1[1] + s1[3]) + 32.f * (d0 * d0 + d1 * d1 + d2 * d2 + d3 * d3);
    float rstd = __builtin_amdgcn_rsqf(M2 * (1.f / 128.f) + 1e-5f);
    u32x4 ho[2], hi_[2];
    #pragma unroll
    for (int e = 0; e < 8; ++e) {
      float v0 = acc[2 * e], v1 = acc[2 * e + 1];
      float y0_ = (v0 - mean) * rstd, y1_ = (v1 - mean) * rstd;
      float g0 = y0_ * __builtin_amdgcn_rcpf(1.f + __builtin_amdgcn_exp2f(-1.44269504f * y0_));
      float g1 = y1_ * __builtin_amdgcn_rcpf(1.f + __builtin_amdgcn_exp2f(-1.44269504f * y1_));
      unsigned hv = cvtpk(g0, g1);
      ho[e >> 2][e & 3] = valid[j] ? hv : 0u;
      hi_[e >> 2][e & 3] = cvtpk(v0, v1);
    }
    if (haddr[j] >= 0) {
      char* hp = lds + L_H + haddr[j] + wid * 64 + hh * 32;
      *(u32x4*)hp = ho[0];
      *(u32x4*)(hp + 16) = ho[1];
    }
    char* cp = lds + L_C + p * 256;
    *(u32x4*)(cp + 16 * ((4 * wid + 2 * hh) ^ (p & 15))) = hi_[0];
    *(u32x4*)(cp + 16 * ((4 * wid + 2 * hh + 1) ^ (p & 15))) = hi_[1];
  }
  __syncthreads();

  f32x16 acc[4];
  #pragma unroll
  for (int s4 = 0; s4 < 4; ++s4) {
    int p = (4 * s4 + (r >> 3) + 1) * 10 + (r & 7) + 1;
    const char* cp = lds + L_C + p * 256;
    u32x4 a0 = *(const u32x4*)(cp + 16 * ((4 * wid + 2 * hh) ^ (p & 15)));
    u32x4 a1 = *(const u32x4*)(cp + 16 * ((4 * wid + 2 * hh + 1) ^ (p & 15)));
    #pragma unroll
    for (int e = 0; e < 4; ++e) {
      acc[s4][2 * e] = bflo(a0[e]); acc[s4][2 * e + 1] = bfhi(a0[e]);
      acc[s4][8 + 2 * e] = bflo(a1[e]); acc[s4][8 + 2 * e + 1] = bfhi(a1[e]);
    }
  }
  const int hbase = L_H + (r >> 3) * H_RS + (r & 7) * H_PS + hh * 16;
  #pragma unroll 1
  for (int t = 0; t < 9; ++t) {
    __syncthreads();
    char* cur = lds + ((t & 1) ? L_C : L_W);
    char* nxt = lds + ((t & 1) ? L_W : L_C);
    {
      const u16* wn = (t < 8) ? wconv + (size_t)(t + 1) * 16384 : wz2;
      stage_w(wn, nxt, wid, lane);
    }
    const int di = t / 3, dj = t - di * 3;
    const char* hb = lds + hbase + di * H_RS + dj * H_PS;
    #pragma unroll
    for (int s = 0; s < 8; ++s) A[s] = tr_frag(cur, trbase + s * 4096);
    #pragma unroll
    for (int s4 = 0; s4 < 4; ++s4)
      #pragma unroll
      for (int s = 0; s < 8; ++s) {
        bf16x8 B = *(const bf16x8*)(hb + s4 * 4 * H_RS + s * 32);
        acc[s4] = MFMA(A[s], B, acc[s4]);
      }
  }
  __syncthreads();
  #pragma unroll
  for (int s4 = 0; s4 < 4; ++s4) {
    u32x4 o0, o1;
    #pragma unroll
    for (int e = 0; e < 4; ++e) { o0[e] = cvtpk(acc[s4][2 * e], acc[s4][2 * e + 1]); o1[e] = cvtpk(acc[s4][8 + 2 * e], acc[s4][8 + 2 * e + 1]); }
    char* hp = lds + L_H + (s4 * 32 + r) * 272 + wid * 64 + hh * 32;
    *(u32x4*)hp = o0; *(u32x4*)(hp + 16) = o1;
  }
  #pragma unroll
  for (int s = 0; s < 8; ++s) A[s] = tr_frag(lds + L_C, trbase + s * 4096);
  u32x4 xfr[8];
  #pragma unroll
  for (int i = 0; i < 8; ++i) {
    int px = i * 16 + (tid >> 4);
    int gy = y0 + (px >> 3), gx = x0 + (px & 7);
    xfr[i] = *(const u32x4*)((const char*)xf + (img + gy * 128 + gx) * 256 + (tid & 15) * 16);
  }
  __syncthreads();
  f32x16 z[4];
  #pragma unroll
  for (int s4 = 0; s4 < 4; ++s4) {
    z[s4] = f32x16{};
    #pragma unroll
    for (int s = 0; s < 8; ++s) {
      bf16x8 B = *(const bf16x8*)(lds + L_H + (s4 * 32 + r) * 272 + s * 32 + hh * 16);
      z[s4] = MFMA(A[s], B, z[s4]);
    }
  }
  #pragma unroll
  for (int s4 = 0; s4 < 4; ++s4) {
    u32x4 o0, o1;
    #pragma unroll
    for (int e = 0; e < 4; ++e) { o0[e] = cvtpk(z[s4][2 * e], z[s4][2 * e + 1]); o1[e] = cvtpk(z[s4][8 + 2 * e], z[s4][8 + 2 * e + 1]); }
    char* op = lds + L_W + (s4 * 32 + r) * 272 + wid * 64 + hh * 32;
    *(u32x4*)op = o0; *(u32x4*)(op + 16) = o1;
  }
  __syncthreads();
  #pragma unroll
  for (int i = 0; i < 8; ++i) {
    int px = i * 16 + (tid >> 4);
    int gy = y0 + (px >> 3), gx = x0 + (px & 7);
    u32x4 zv = *(const u32x4*)(lds + L_W + px * 272 + (tid & 15) * 16);
    u32x4 o;
    #pragma unroll
    for (int e = 0; e < 4; ++e) o[e] = cvtpk(bflo(zv[e]) + bflo(xfr[i][e]), bfhi(zv[e]) + bfhi(xfr[i][e]));
    *(u32x4*)((char*)out + (img + gy * 128 + gx) * 256 + (tid & 15) * 16) = o;
  }
}

extern "C" void kernel_launch(void* const* d_in, const int* in_sizes, int n_in,
                              void* d_out, int out_size, void* d_ws, size_t ws_size,
                              hipStream_t stream) {
  constexpr int SH = L_TOTAL + 4096;
  hipFuncSetAttribute((const void*)cn_kernel, hipFuncAttributeMaxDynamicSharedMemorySize, SH);
  cn_kernel<<<256, 256, SH, stream>>>((const u16*)d_in[0], (const u16*)d_in[1], (const u16*)d_in[2], (const u16*)d_in[3],
                                      (const u16*)d_in[4], (const u16*)d_in[5], (u16*)d_out);
}
```

```cpp
#include <hip/hip_runtime.h>
#include <stdint.h>

typedef unsigned short u16;
typedef short s16x4 __attribute__((ext_vector_type(4)));
typedef short s16x8 __attribute__((ext_vector_type(8)));
typedef __bf16 bf16x8 __attribute__((ext_vector_type(8)));
typedef __bf16 bf16x2 __attribute__((ext_vector_type(2)));
typedef float f32x2 __attribute__((ext_vector_type(2)));
typedef float f32x4 __attribute__((ext_vector_type(4)));
typedef float f32x16 __attribute__((ext_vector_type(16)));
typedef unsigned u32x4 __attribute__((ext_vector_type(4)));
#define DI __device__ __forceinline__
#define LDSP(T) __attribute__((address_space(3))) T*

constexpr int H_RS = 2944, H_PS = 272;
constexpr int L_H = 0;
constexpr int L_C = 52992;
constexpr int L_W = L_C + 49152;
constexpr int L_ST = L_W + 32768;
constexpr int L_TOTAL = L_ST + 2048;

DI unsigned cvtpk(float lo, float hi) { f32x2 v = {lo, hi}; bf16x2 b = __builtin_convertvector(v, bf16x2); return __builtin_bit_cast(unsigned, b); }
DI float bflo(unsigned u) { return __uint_as_float(u << 16); }
DI float bfhi(unsigned u) { return __uint_as_float(u & 0xffff0000u); }
DI float swap_add(float v) {
  auto r = __builtin_amdgcn_permlane32_swap(__float_as_uint(v), __float_as_uint(v), false, false);
  return __uint_as_float(r[0]) + __uint_as_float(r[1]);
}
DI void glds16(const void* g, char* lds_wave_base) {
  __builtin_amdgcn_global_load_lds((const __attribute__((address_space(1))) unsigned*)g, (LDSP(unsigned))lds_wave_base, 16, 0, 0);
}
DI bf16x8 tr_frag(const char* lds, int off) {
  s16x4 lo = __builtin_amdgcn_ds_read_tr16_b64_v4i16((LDSP(s16x4))(lds + off));
  s16x4 hi = __builtin_amdgcn_ds_read_tr16_b64_v4i16((LDSP(s16x4))(lds + off + 1024));
  return __builtin_bit_cast(bf16x8, __builtin_shufflevector(lo, hi, 0, 1, 2, 3, 4, 5, 6, 7));
}
#define ST(k)
#define MFMA(a, b, c) __builtin_amdgcn_mfma_f32_32x32x16_bf16((a), (b), (c), 0, 0, 0)

DI void stage_w(const u16* w, char* dst, int wid, int lane) {
  const int chunk = (lane & 15) ^ (4 * (lane >> 4));
  const char* src = (const char*)w + (lane >> 4) * 256 + chunk * 16;
  #pragma unroll
  for (int i = 0; i < 8; ++i) {
    int piece = wid * 8 + i;
    glds16(src + piece * 1024, dst + piece * 1024);
  }
}

__global__ __launch_bounds__(256, 1) void cn_kernel(const u16* __restrict__ xf, const u16* __restrict__ x, const u16* __restrict__ c,
                                                    const u16* __restrict__ wz1, const u16* __restrict__ wz2, const u16* __restrict__ wconv,
                                                    u16* __restrict__ out, unsigned long long* __restrict__ ws) {
  extern __shared__ __attribute__((aligned(16))) char lds[];
  const int tid = threadIdx.x, lane = tid & 63, wid = __builtin_amdgcn_readfirstlane(tid >> 6);
  const int r = lane & 31, hh = lane >> 5;
  const int blk = blockIdx.x, xcd = blk & 7, idx = blk >> 3;
  const int b = xcd >> 2, y0 = (xcd & 3) * 32 + (idx >> 4) * 16, x0 = (idx & 15) * 8;
  const size_t img = (size_t)b * 128 * 128;

  stage_w(wz1, lds + L_W, wid, lane);
  #pragma unroll
  for (int i = 0; i < 12; ++i) {
    int piece = wid * 12 + i;
    int p = piece * 4 + (lane >> 4);
    int pp = p < 180 ? p : 179;
    int row = pp / 10, col = pp - row * 10;
    int gy = min(max(y0 - 1 + row, 0), 127), gx = min(max(x0 - 1 + col, 0), 127);
    int chunk = (lane & 15) ^ (p & 15);
    glds16((const char*)c + (img + gy * 128 + gx) * 256 + chunk * 16, lds + L_C + piece * 1024);
  }
  u32x4 xr[6][2];
  bool valid[6];
  int haddr[6];
  #pragma unroll
  for (int j = 0; j < 6; ++j) {
    int p = j * 32 + r;
    int pp = p < 180 ? p : 179;
    int row = pp / 10, col = pp - row * 10;
    int gy = y0 - 1 + row, gx = x0 - 1 + col;
    valid[j] = gy >= 0 && gy < 128 && gx >= 0 && gx < 128;
    haddr[j] = p < 180 ? row * H_RS + col * H_PS : -1;
    gy = min(max(gy, 0), 127); gx = min(max(gx, 0), 127);
    const char* src = (const char*)x + (img + gy * 128 + gx) * 256 + wid * 64 + hh * 32;
    xr[j][0] = *(const u32x4*)src;
    xr[j][1] = *(const u32x4*)(src + 16);
  }
  ST(1);
  __syncthreads();
  ST(2);
  const int q = (lane & 15) >> 2, p4 = lane & 3, blkb = (lane >> 4) & 1;
  const int trbase = (8 * hh + q) * 256 + 64 * (wid ^ q) + 32 * (p4 & 1) + 16 * blkb + 8 * (p4 >> 1);
  bf16x8 A[8];
  #pragma unroll
  for (int s = 0; s < 8; ++s) A[s] = tr_frag(lds + L_W, trbase + s * 4096);
  __syncthreads();
  stage_w(wconv, lds + L_W, wid, lane);

  ST(3);
  #pragma unroll
  for (int j = 0; j < 6; ++j) {
    const int p = j * 32 + r;
    f32x16 acc;
    #pragma unroll
    for (int e = 0; e < 4; ++e) {
      acc[2 * e] = bflo(xr[j][0][e]); acc[2 * e + 1] = bfhi(xr[j][0][e]);
      acc[8 + 2 * e] = bflo(xr[j][1][e]); acc[8 + 2 * e + 1] = bfhi(xr[j][1][e]);
    }
    #pragma unroll
    for (int s = 0; s < 8; ++s) {
      bf16x8 B = *(const bf16x8*)(lds + L_C + p * 256 + 16 * ((2 * s + hh) ^ (p & 15)));
      acc = MFMA(A[s], B, acc);
    }
    float sum = 0.f;
    #pragma unroll
    for (int i = 0; i < 16; ++i) sum += acc[i];
    sum = swap_add(sum);
    float mw = sum * (1.f / 32.f);
    float qq = 0.f;
    #pragma unroll
    for (int i = 0; i < 16; ++i) { float d = acc[i] - mw; qq += d * d; }
    qq = swap_add(qq);
    char* st = lds + L_ST + (j & 1) * 1024;
    if (hh == 0) *(f32x2*)(st + r * 32 + wid * 8) = f32x2{mw, qq};
    asm volatile("s_waitcnt lgkmcnt(0)" ::: "memory");
    __builtin_amdgcn_s_barrier();
    f32x4 s0 = *(const f32x4*)(st + r * 32), s1 = *(const f32x4*)(st + r * 32 + 16);
    float mean = (s0[0] + s0[2] + s1[0] + s1[2]) * 0.25f;
    float d0 = s0[0] - mean, d1 = s0[2] - mean, d2 = s1[0] - mean, d3 = s1[2] - mean;
    float M2 = (s0[1] + s0[3] + s1[1] + s1[3]) + 32.f * (d0 * d0 + d1 * d1 + d2 * d2 + d3 * d3);
    float rstd = __builtin_amdgcn_rsqf(M2 * (1.f / 128.f) + 1e-5f);
    u32x4 ho[2], hi_[2];
    #pragma unroll
    for (int e = 0; e < 8; ++e) {
      float v0 = acc[2 * e], v1 = acc[2 * e + 1];
      float y0_ = (v0 - mean) * rstd, y1_ = (v1 - mean) * rstd;
      float g0 = y0_ * __builtin_amdgcn_rcpf(1.f + __builtin_amdgcn_exp2f(-1.44269504f * y0_));
      float g1 = y1_ * __builtin_amdgcn_rcpf(1.f + __builtin_amdgcn_exp2f(-1.44269504f * y1_));
      unsigned hv = cvtpk(g0, g1);
      ho[e >> 2][e & 3] = valid[j] ? hv : 0u;
      hi_[e >> 2][e & 3] = cvtpk(v0, v1);
    }
    if (haddr[j] >= 0) {
      char* hp = lds + L_H + haddr[j] + wid * 64 + hh * 32;
      *(u32x4*)hp = ho[0];
      *(u32x4*)(hp + 16) = ho[1];
    }
    char* cp = lds + L_C + p * 256;
    *(u32x4*)(cp + 16 * ((4 * wid + 2 * hh) ^ (p & 15))) = hi_[0];
    *(u32x4*)(cp + 16 * ((4 * wid + 2 * hh + 1) ^ (p & 15))) = hi_[1];
  }
  __syncthreads();

  ST(4);
  f32x16 acc[4];
  #pragma unroll
  for (int s4 = 0; s4 < 4; ++s4) {
    int p = (4 * s4 + (r >> 3) + 1) * 10 + (r & 7) + 1;
    const char* cp = lds + L_C + p * 256;
    u32x4 a0 = *(const u32x4*)(cp + 16 * ((4 * wid + 2 * hh) ^ (p & 15)));
    u32x4 a1 = *(const u32x4*)(cp + 16 * ((4 * wid + 2 * hh + 1) ^ (p & 15)));
    #pragma unroll
    for (int e = 0; e < 4; ++e) {
      acc[s4][2 * e] = bflo(a0[e]); acc[s4][2 * e + 1] = bfhi(a0[e]);
      acc[s4][8 + 2 * e] = bflo(a1[e]); acc[s4][8 + 2 * e + 1] = bfhi(a1[e]);
    }
  }
  const int hbase = L_H + (r >> 3) * H_RS + (r & 7) * H_PS + hh * 16;
  #pragma unroll 1
  for (int t = 0; t < 9; ++t) {
    __syncthreads();
    char* cur = lds + ((t & 1) ? L_C : L_W);
    char* nxt = lds + ((t & 1) ? L_W : L_C);
    {
      const u16* wn = (t < 8) ? wconv + (size_t)(t + 1) * 16384 : wz2;
      stage_w(wn, nxt, wid, lane);
    }
    const int di = t / 3, dj = t - di * 3;
    const char* hb = lds + hbase + di * H_RS + dj * H_PS;
    #pragma unroll
    for (int s = 0; s < 8; ++s) A[s] = tr_frag(cur, trbase + s * 4096);
    #pragma unroll
    for (int s4 = 0; s4 < 4; ++s4)
      #pragma unroll
      for (int s = 0; s < 8; ++s) {
        bf16x8 B = *(const bf16x8*)(hb + s4 * 4 * H_RS + s * 32);
        acc[s4] = MFMA(A[s], B, acc[s4]);
      }
  }
  ST(5);
  __syncthreads();
  #pragma unroll
  for (int s4 = 0; s4 < 4; ++s4) {
    u32x4 o0, o1;
    #pragma unroll
    for (int e = 0; e < 4; ++e) { o0[e] = cvtpk(acc[s4][2 * e], acc[s4][2 * e + 1]); o1[e] = cvtpk(acc[s4][8 + 2 * e], acc[s4][8 + 2 * e + 1]); }
    char* hp = lds + L_H + (s4 * 32 + r) * 272 + wid * 64 + hh * 32;
    *(u32x4*)hp = o0; *(u32x4*)(hp + 16) = o1;
  }
  #pragma unroll
  for (int s = 0; s < 8; ++s) A[s] = tr_frag(lds + L_C, trbase + s * 4096);
  u32x4 xfr[8];
  #pragma unroll
  for (int i = 0; i < 8; ++i) {
    int px = i * 16 + (tid >> 4);
    int gy = y0 + (px >> 3), gx = x0 + (px & 7);
    xfr[i] = *(const u32x4*)((const char*)xf + (img + gy * 128 + gx) * 256 + (tid & 15) * 16);
  }
  __syncthreads();
  ST(6);
  f32x16 z[4];
  #pragma unroll
  for (int s4 = 0; s4 < 4; ++s4) {
    z[s4] = f32x16{};
    #pragma unroll
    for (int s = 0; s < 8; ++s) {
      bf16x8 B = *(const bf16x8*)(lds + L_H + (s4 * 32 + r) * 272 + s * 32 + hh * 16);
      z[s4] = MFMA(A[s], B, z[s4]);
    }
  }
  #pragma unroll
  for (int s4 = 0; s4 < 4; ++s4) {
    u32x4 o0, o1;
    #pragma unroll
    for (int e = 0; e < 4; ++e) { o0[e] = cvtpk(z[s4][2 * e], z[s4][2 * e + 1]); o1[e] = cvtpk(z[s4][8 + 2 * e], z[s4][8 + 2 * e + 1]); }
    char* op = lds + L_W + (s4 * 32 + r) * 272 + wid * 64 + hh * 32;
    *(u32x4*)op = o0; *(u32x4*)(op + 16) = o1;
  }
  __syncthreads();
  ST(7);
  #pragma unroll
  for (int i = 0; i < 8; ++i) {
    int px = i * 16 + (tid >> 4);
    int gy = y0 + (px >> 3), gx = x0 + (px & 7);
    u32x4 zv = *(const u32x4*)(lds + L_W + px * 272 + (tid & 15) * 16);
    u32x4 o;
    #pragma unroll
    for (int e = 0; e < 4; ++e) o[e] = cvtpk(bflo(zv[e]) + bflo(xfr[i][e]), bfhi(zv[e]) + bfhi(xfr[i][e]));
    *(u32x4*)((char*)out + (img + gy * 128 + gx) * 256 + (tid & 15) * 16) = o;
  }
}

extern "C" void kernel_launch(void* const* d_in, const int* in_sizes, int n_in,
                              void* d_out, int out_size, void* d_ws, size_t ws_size,
                              hipStream_t stream) {
  constexpr int SH = L_TOTAL + 4096;
  hipFuncSetAttribute((const void*)cn_kernel, hipFuncAttributeMaxDynamicSharedMemorySize, SH);
  cn_kernel<<<256, 256, SH, stream>>>((const u16*)d_in[0], (const u16*)d_in[1], (const u16*)d_in[2], (const u16*)d_in[3],
                                      (const u16*)d_in[4], (const u16*)d_in[5], (u16*)d_out, (unsigned long long*)d_ws);
}
```

```cpp
#include <hip/hip_runtime.h>
#include <stdint.h>

__global__ __launch_bounds__(512, 1) void cn_kernel(const void* xf, const void* x, const void* c, const void* w1,
                                                    const void* w2, const void* wc, void* out, void* ws) {
  asm volatile(
    "s_mov_b64 s[0:1], %0\n\t"
    "s_mov_b32 s2, %1\n\t"
    "v_mov_b32 v0, %2\n\t"
    "s_load_dwordx16 s[4:19], s[0:1], 0x0\n\t"
    "s_setprio 3\n\t"
    "s_mov_b32 s68, s2\n\t"
    "v_lshrrev_b32 v199, 6, v0\n\t"
    "v_and_b32 v192, 63, v0\n\t"
    "v_readfirstlane_b32 s69, v199\n\t"
    "s_and_b32 s77, s68, 7\n\t"
    "s_lshr_b32 s78, s68, 3\n\t"
    "s_and_b32 s79, s77, 3\n\t"
    "s_lshl_b32 s74, s79, 5\n\t"
    "s_lshr_b32 s79, s78, 3\n\t"
    "s_lshl_b32 s79, s79, 3\n\t"
    "s_add_u32 s74, s74, s79\n\t"
    "s_and_b32 s79, s78, 7\n\t"
    "s_lshl_b32 s75, s79, 4\n\t"
    "s_lshr_b32 s79, s77, 2\n\t"
    "s_lshl_b32 s79, s79, 7\n\t"
    "s_add_u32 s79, s79, s74\n\t"
    "s_lshl_b32 s79, s79, 7\n\t"
    "s_add_u32 s79, s79, s75\n\t"
    "s_lshl_b32 s73, s79, 8\n\t"
    "s_sub_u32 s72, s73, 0x8100\n\t"
    "v_lshrrev_b32 v199, 4, v192\n\t"
    "v_and_b32 v200, 3, v192\n\t"
    "v_bfe_u32 v203, v192, 2, 2\n\t"
    "s_and_b32 s70, s69, 3\n\t"
    "s_lshr_b32 s71, s69, 2\n\t"
    "s_mul_i32 s76, s69, 0x440\n\t"
    "s_mul_i32 s83, s69, 0x410\n\t"
    "v_lshlrev_b32 v210, 6, v199\n\t"
    "v_lshl_add_u32 v201, v200, 4, v210\n\t"
    "v_subrev_u32 v200, s70, v200\n\t"
    "v_and_b32 v200, 3, v200\n\t"
    "v_lshl_add_u32 v202, v200, 4, v210\n\t"
    "s_lshl_b32 s77, s69, 2\n\t"
    "v_add_u32 v203, s77, v203\n\t"
    "v_mul_u32_u24 v208, 0xe39, v203\n\t"
    "v_lshrrev_b32 v208, 16, v208\n\t"
    "v_mul_u32_u24 v209, 18, v208\n\t"
    "v_sub_u32 v209, v203, v209\n\t"
    "v_lshl_add_u32 v209, v208, 7, v209\n\t"
    "v_lshlrev_b32 v209, 8, v209\n\t"
    "v_add_u32 v212, s72, v209\n\t"
    "v_add_u32 v207, 32, v203\n\t"
    "v_mul_u32_u24 v208, 0xe39, v207\n\t"
    "v_lshrrev_b32 v208, 16, v208\n\t"
    "v_mul_u32_u24 v209, 18, v208\n\t"
    "v_sub_u32 v209, v207, v209\n\t"
    "v_lshl_add_u32 v209, v208, 7, v209\n\t"
    "v_lshlrev_b32 v209, 8, v209\n\t"
    "v_add_u32 v213, s72, v209\n\t"
    "v_lshrrev_b32 v199, 4, v192\n\t"
    "v_lshlrev_b32 v200, 2, v199\n\t"
    "v_and_b32 v24, 15, v192\n\t"
    "v_xor_b32 v24, v24, v200\n\t"
    "v_lshlrev_b32 v24, 4, v24\n\t"
    "v_lshl_add_u32 v24, v199, 8, v24\n\t"
    "v_lshrrev_b32 v199, 2, v192\n\t"
    "v_and_b32 v200, 3, v192\n\t"
    "v_lshlrev_b32 v200, 4, v200\n\t"
    "v_lshl_add_u32 v199, v199, 8, v200\n\t"
    "s_lshl_b32 s77, s70, 6\n\t"
    "s_lshl_b32 s78, s71, 12\n\t"
    "s_add_u32 s77, s77, s78\n\t"
    "v_add_u32 v15, s77, v199\n\t"
    "s_mul_i32 s81, s69, 0xc00\n\t"
    "s_add_u32 s81, s81, 0x20f00\n\t"
    "s_lshl_b32 s22, s69, 12\n\t"
    "s_add_u32 s84, s22, 0x18f00\n\t"
    "s_add_u32 s85, s76, 0x8800\n\t"
    "s_add_u32 s23, s83, 0x14e00\n\t"
    "s_waitcnt lgkmcnt(0)\n\t"
    "s_mov_b64 s[44:45], s[8:9]\n\t"
    "s_mov_b64 s[40:41], s[6:7]\n\t"
    "s_mov_b64 s[56:57], s[14:15]\n\t"
    "s_mov_b64 s[52:53], s[12:13]\n\t"
    "s_mov_b64 s[48:49], s[10:11]\n\t"
    "s_and_b32 s45, s45, 0xffff\n\t"
    "s_mov_b32 s46, 0x800000\n\t"
    "s_mov_b32 s47, 0x20000\n\t"
    "s_and_b32 s41, s41, 0xffff\n\t"
    "s_mov_b32 s42, 0x800000\n\t"
    "s_mov_b32 s43, 0x20000\n\t"
    "s_and_b32 s57, s57, 0xffff\n\t"
    "s_mov_b32 s58, 0x48000\n\t"
    "s_mov_b32 s59, 0x20000\n\t"
    "s_and_b32 s53, s53, 0xffff\n\t"
    "s_mov_b32 s54, 0x8000\n\t"
    "s_mov_b32 s55, 0x20000\n\t"
    "s_and_b32 s49, s49, 0xffff\n\t"
    "s_mov_b32 s50, 0x8000\n\t"
    "s_mov_b32 s51, 0x20000\n\t"
    "s_lshr_b32 s77, s68, 3\n\t"
    "s_mul_i32 s78, s77, 0x2400\n\t"
    "v_lshlrev_b32 v199, 5, v0\n\t"
    "buffer_load_dword v224, v199, s[56:59], s78 offen\n\t"
    "s_lshl_b32 s78, s77, 10\n\t"
    "v_and_b32 v200, 0x3ff, v199\n\t"
    "buffer_load_dword v225, v200, s[52:55], s78 offen\n\t"
    "v_add_u32 v207, v212, v202\n\t"
    "s_add_u32 m0, s76, 0\n\t"
    "s_nop 0\n\t"
    "buffer_load_dwordx4 v207, s[44:47], 0 offen nt lds\n\t"
    "v_add_u32 v208, v212, v201\n\t"
    "s_add_u32 m0, s83, 0xcc00\n\t"
    "s_nop 0\n\t"
    "buffer_load_dwordx4 v208, s[40:43], 0 offen nt lds\n\t"
    "v_add_u32 v207, v213, v202\n\t"
    "s_add_u32 m0, s76, 0x2200\n\t"
    "s_nop 0\n\t"
    "buffer_load_dwordx4 v207, s[44:47], 0 offen nt lds\n\t"
    "v_add_u32 v208, v213, v201\n\t"
    "s_add_u32 m0, s83, 0xec80\n\t"
    "s_nop 0\n\t"
    "buffer_load_dwordx4 v208, s[40:43], 0 offen nt lds\n\t"
    "s_barrier\n\t"
    "s_add_u32 s82, s22, 0\n\t"
    "s_add_u32 m0, s82, 0x18f00\n\t"
    "s_nop 0\n\t"
    "buffer_load_dwordx4 v24, s[48:51], s82 offen lds\n\t"
    "s_add_u32 s82, s22, 0x400\n\t"
    "s_add_u32 m0, s82, 0x18f00\n\t"
    "s_nop 0\n\t"
    "buffer_load_dwordx4 v24, s[48:51], s82 offen lds\n\t"
    "s_add_u32 s82, s22, 0x800\n\t"
    "s_add_u32 m0, s82, 0x18f00\n\t"
    "s_nop 0\n\t"
    "buffer_load_dwordx4 v24, s[48:51], s82 offen lds\n\t"
    "s_add_u32 s82, s22, 0xc00\n\t"
    "s_add_u32 m0, s82, 0x18f00\n\t"
    "s_nop 0\n\t"
    "buffer_load_dwordx4 v24, s[48:51], s82 offen lds\n\t"
    "s_setprio 0\n\t"
    "v_and_b32 v193, 31, v192\n\t"
    "v_lshrrev_b32 v194, 5, v192\n\t"
    "v_lshrrev_b32 v195, 2, v193\n\t"
    "s_mul_i32 s77, s71, 0x2200\n\t"
    "s_mul_i32 s80, s71, 0x2080\n\t"
    "v_and_b32 v199, 3, v193\n\t"
    "v_lshlrev_b32 v199, 6, v199\n\t"
    "v_mul_u32_u24 v210, 0x440, v195\n\t"
    "v_add3_u32 v210, v210, v199, s77\n\t"
    "v_add_u32 v200, v194, v195\n\t"
    "v_and_b32 v211, 3, v200\n\t"
    "v_lshl_add_u32 v1, v211, 4, v210\n\t"
    "v_xor_b32 v211, 2, v211\n\t"
    "v_lshl_add_u32 v2, v211, 4, v210\n\t"
    "v_mul_u32_u24 v210, 0x410, v195\n\t"
    "v_add_u32 v210, v210, v199\n\t"
    "v_lshl_add_u32 v210, v194, 5, v210\n\t"
    "s_lshl_b32 s78, s70, 8\n\t"
    "s_add_u32 s79, s78, s80\n\t"
    "s_add_u32 s79, s79, 0xcc00\n\t"
    "v_add_u32 v4, s79, v210\n\t"
    "v_bfe_u32 v204, v192, 2, 2\n\t"
    "v_and_b32 v205, 3, v192\n\t"
    "v_bfe_u32 v206, v192, 4, 1\n\t"
    "v_and_b32 v199, 1, v205\n\t"
    "v_lshlrev_b32 v199, 5, v199\n\t"
    "v_lshrrev_b32 v200, 1, v205\n\t"
    "v_lshl_add_u32 v199, v200, 3, v199\n\t"
    "v_lshl_add_u32 v211, v194, 3, v204\n\t"
    "v_xor_b32 v200, s70, v204\n\t"
    "v_lshl_add_u32 v200, v200, 6, v199\n\t"
    "v_lshl_add_u32 v200, v206, 4, v200\n\t"
    "v_lshl_add_u32 v200, v211, 8, v200\n\t"
    "v_add_u32 v13, 0x18f00, v200\n\t"
    "v_xor_b32 v200, s71, v206\n\t"
    "v_lshl_add_u32 v200, v200, 4, v199\n\t"
    "v_lshl_add_u32 v200, v211, 6, v200\n\t"
    "v_add_u32 v14, s81, v200\n\t"
    "v_add_u32 v25, s84, v200\n\t"
    "v_add_u32 v220, s85, v200\n\t"
    "v_add_u32 v221, s23, v200\n\t"
    "v_lshlrev_b32 v199, 5, v193\n\t"
    "s_lshl_b32 s79, s71, 10\n\t"
    "s_add_u32 s79, s79, 0x26f00\n\t"
    "v_add_u32 v12, s79, v199\n\t"
    "s_lshl_b32 s80, s70, 3\n\t"
    "v_add_u32 v11, s80, v12\n\t"
    "v_mul_u32_u24 v210, 0x110, v193\n\t"
    "v_lshl_add_u32 v211, v194, 5, v210\n\t"
    "s_lshl_b32 s78, s70, 6\n\t"
    "s_add_u32 s79, s78, s77\n\t"
    "v_add_u32 v3, s79, v211\n\t"
    "s_lshl_b32 s79, s71, 4\n\t"
    "s_add_u32 s79, s79, s78\n\t"
    "v_add_u32 v5, s79, v211\n\t"
    "v_lshl_add_u32 v211, v194, 4, v210\n\t"
    "v_add_u32 v7, s77, v211\n\t"
    "s_lshl_b32 s77, s71, 5\n\t"
    "s_add_u32 s77, s77, 0\n\t"
    "v_add_u32 v207, s77, v193\n\t"
    "v_mul_u32_u24 v208, 0xe39, v207\n\t"
    "v_lshrrev_b32 v208, 16, v208\n\t"
    "v_mul_u32_u24 v209, 18, v208\n\t"
    "v_sub_u32 v209, v207, v209\n\t"
    "s_sub_u32 s77, s74, 1\n\t"
    "v_add_u32 v208, s77, v208\n\t"
    "s_sub_u32 s77, s75, 1\n\t"
    "v_add_u32 v209, s77, v209\n\t"
    "v_or_b32 v208, v208, v209\n\t"
    "v_cmp_gt_u32 vcc, 0x80, v208\n\t"
    "s_nop 1\n\t"
    "v_cndmask_b32 v18, 0, 1.0, vcc\n\t"
    "v_add_u32 v207, 64, v203\n\t"
    "v_mul_u32_u24 v208, 0xe39, v207\n\t"
    "v_lshrrev_b32 v208, 16, v208\n\t"
    "v_mul_u32_u24 v209, 18, v208\n\t"
    "v_sub_u32 v209, v207, v209\n\t"
    "v_lshl_add_u32 v209, v208, 7, v209\n\t"
    "v_lshlrev_b32 v209, 8, v209\n\t"
    "v_add_u32 v214, s72, v209\n\t"
    "v_add_u32 v72, v214, v202\n\t"
    "v_add_u32 v73, v214, v201\n\t"
    "v_add_u32 v207, 0x60, v203\n\t"
    "v_mul_u32_u24 v208, 0xe39, v207\n\t"
    "v_lshrrev_b32 v208, 16, v208\n\t"
    "v_mul_u32_u24 v209, 18, v208\n\t"
    "v_sub_u32 v209, v207, v209\n\t"
    "v_lshl_add_u32 v209, v208, 7, v209\n\t"
    "v_lshlrev_b32 v209, 8, v209\n\t"
    "v_add_u32 v215, s72, v209\n\t"
    "v_add_u32 v74, v215, v202\n\t"
    "v_add_u32 v75, v215, v201\n\t"
    "s_mov_b64 s[36:37], s[4:5]\n\t"
    "s_mov_b64 s[60:61], s[16:17]\n\t"
    "s_mov_b64 s[64:65], s[18:19]\n\t"
    "s_and_b32 s37, s37, 0xffff\n\t"
    "s_mov_b32 s38, 0x800000\n\t"
    "s_mov_b32 s39, 0x20000\n\t"
    "s_and_b32 s61, s61, 0xffff\n\t"
    "s_mov_b32 s62, 0x800000\n\t"
    "s_mov_b32 s63, 0x20000\n\t"
    "s_and_b32 s65, s65, 0xffff\n\t"
    "s_mov_b32 s66, 0x10000000\n\t"
    "s_mov_b32 s67, 0x20000\n\t"
    "v_add_u32 v207, 0x80, v203\n\t"
    "v_mul_u32_u24 v208, 0xe39, v207\n\t"
    "v_lshrrev_b32 v208, 16, v208\n\t"
    "v_mul_u32_u24 v209, 18, v208\n\t"
    "v_sub_u32 v209, v207, v209\n\t"
    "v_lshl_add_u32 v209, v208, 7, v209\n\t"
    "v_lshlrev_b32 v209, 8, v209\n\t"
    "v_add_u32 v216, s72, v209\n\t"
    "v_add_u32 v76, v216, v202\n\t"
    "v_add_u32 v77, v216, v201\n\t"
    "v_add_u32 v207, 0xa0, v203\n\t"
    "v_mul_u32_u24 v208, 0xe39, v207\n\t"
    "v_lshrrev_b32 v208, 16, v208\n\t"
    "v_mul_u32_u24 v209, 18, v208\n\t"
    "v_sub_u32 v209, v207, v209\n\t"
    "v_lshl_add_u32 v209, v208, 7, v209\n\t"
    "v_lshlrev_b32 v209, 8, v209\n\t"
    "v_add_u32 v217, s72, v209\n\t"
    "v_add_u32 v78, v217, v202\n\t"
    "v_add_u32 v79, v217, v201\n\t"
    "v_lshrrev_b32 v196, 1, v195\n\t"
    "v_xor_b32 v196, v196, v195\n\t"
    "v_lshrrev_b32 v199, 2, v195\n\t"
    "v_xor_b32 v196, v196, v199\n\t"
    "v_and_b32 v196, 1, v196\n\t"
    "v_add_u32 v199, 1, v195\n\t"
    "v_lshrrev_b32 v199, 1, v199\n\t"
    "v_lshlrev_b32 v199, 2, v199\n\t"
    "v_sub_u32 v197, v193, v199\n\t"
    "v_mad_u32_u24 v198, v196, 18, v197\n\t"
    "v_mul_u32_u24 v210, 0x110, v198\n\t"
    "v_lshl_add_u32 v210, v194, 4, v210\n\t"
    "s_lshl_b32 s79, s71, 5\n\t"
    "v_add_u32 v6, s79, v210\n\t"
    "v_add_u32 v199, 3, v198\n\t"
    "v_lshrrev_b32 v200, 2, v199\n\t"
    "v_and_b32 v199, 3, v199\n\t"
    "v_lshlrev_b32 v199, 6, v199\n\t"
    "v_mul_u32_u24 v200, 0x410, v200\n\t"
    "v_add_u32 v199, v199, v200\n\t"
    "v_lshl_add_u32 v199, v194, 5, v199\n\t"
    "s_add_u32 m0, s81, 0\n\t"
    "s_mov_b32 s82, 0\n\t"
    "buffer_load_dwordx4 v15, s[56:59], s82 offen lds\n\t"
    "s_lshl_b32 s79, s71, 4\n\t"
    "s_lshl_b32 s78, s70, 8\n\t"
    "s_add_u32 s79, s79, s78\n\t"
    "s_add_u32 s79, s79, 0xcc00\n\t"
    "v_add_u32 v8, s79, v199\n\t"
    "v_lshlrev_b32 v199, 4, v192\n\t"
    "v_add_u32 v9, s81, v199\n\t"
    "s_xor_b32 s79, s69, 4\n\t"
    "s_mul_i32 s79, s79, 0xc00\n\t"
    "s_add_u32 s80, s79, 0x20f00\n\t"
    "v_add_u32 v10, s80, v199\n\t"
    "s_add_u32 s80, s83, 0xcc00\n\t"
    "v_add_u32 v17, s80, v199\n\t"
    "s_lshr_b32 s79, s69, 1\n\t"
    "s_xor_b32 s80, s79, s69\n\t"
    "s_lshr_b32 s77, s69, 2\n\t"
    "s_xor_b32 s80, s80, s77\n\t"
    "s_and_b32 s80, s80, 1\n\t"
    "s_lshl_b32 s80, s80, 7\n\t"
    "s_lshl_b32 s79, s79, 2\n\t"
    "s_add_u32 s80, s80, s79\n\t"
    "v_bfe_u32 v199, v192, 2, 2\n\t"
    "v_add_u32 v199, s80, v199\n\t"
    "v_lshl_add_u32 v16, v199, 8, v201\n\t"
    "s_lshl_b32 s77, s71, 5\n\t"
    "s_add_u32 s77, s77, 64\n\t"
    "v_add_u32 v207, s77, v193\n\t"
    "v_mul_u32_u24 v208, 0xe39, v207\n\t"
    "v_lshrrev_b32 v208, 16, v208\n\t"
    "v_mul_u32_u24 v209, 18, v208\n\t"
    "v_sub_u32 v209, v207, v209\n\t"
    "s_sub_u32 s77, s74, 1\n\t"
    "v_add_u32 v208, s77, v208\n\t"
    "s_sub_u32 s77, s75, 1\n\t"
    "v_add_u32 v209, s77, v209\n\t"
    "v_or_b32 v208, v208, v209\n\t"
    "v_cmp_gt_u32 vcc, 0x80, v208\n\t"
    "s_nop 1\n\t"
    "v_cndmask_b32 v19, 0, 1.0, vcc\n\t"
    "s_lshl_b32 s77, s71, 5\n\t"
    "s_add_u32 s77, s77, 0x80\n\t"
    "v_add_u32 v207, s77, v193\n\t"
    "s_add_u32 m0, s81, 0x400\n\t"
    "s_mov_b32 s82, 0x2000\n\t"
    "buffer_load_dwordx4 v15, s[56:59], s82 offen lds\n\t"
    "v_mul_u32_u24 v208, 0xe39, v207\n\t"
    "v_lshrrev_b32 v208, 16, v208\n\t"
    "v_mul_u32_u24 v209, 18, v208\n\t"
    "v_sub_u32 v209, v207, v209\n\t"
    "s_sub_u32 s77, s74, 1\n\t"
    "v_add_u32 v208, s77, v208\n\t"
    "s_sub_u32 s77, s75, 1\n\t"
    "v_add_u32 v209, s77, v209\n\t"
    "v_or_b32 v208, v208, v209\n\t"
    "v_cmp_gt_u32 vcc, 0x80, v208\n\t"
    "s_nop 1\n\t"
    "v_cndmask_b32 v20, 0, 1.0, vcc\n\t"
    "s_waitcnt vmcnt(0)\n\t"
    "s_barrier\n\t"
    "ds_read_b128 v[44:47], v4\n\t"
    "ds_read_b128 v[52:55], v4 offset:16\n\t"
    "ds_read_b128 v[88:91], v1\n\t"
    "s_add_u32 m0, s81, 0x800\n\t"
    "s_mov_b32 s82, 0x4000\n\t"
    "buffer_load_dwordx4 v15, s[56:59], s82 offen lds\n\t"
    "ds_read_b128 v[92:95], v2\n\t"
    "ds_read_b128 v[96:99], v1 offset:256\n\t"
    "ds_read_b128 v[100:103], v2 offset:256\n\t"
    "ds_read_b128 v[104:107], v1 offset:512\n\t"
    "ds_read_b64_tr_b16 v[224:225], v13\n\t"
    "ds_read_b64_tr_b16 v[226:227], v13 offset:1024\n\t"
    "ds_read_b64_tr_b16 v[228:229], v13 offset:4096\n\t"
    "ds_read_b64_tr_b16 v[230:231], v13 offset:5120\n\t"
    "ds_read_b64_tr_b16 v[232:233], v13 offset:8192\n\t"
    "ds_read_b64_tr_b16 v[234:235], v13 offset:9216\n\t"
    "ds_read_b64_tr_b16 v[236:237], v13 offset:12288\n\t"
    "ds_read_b64_tr_b16 v[238:239], v13 offset:13312\n\t"
    "ds_read_b64_tr_b16 v[240:241], v13 offset:16384\n\t"
    "ds_read_b64_tr_b16 v[242:243], v13 offset:17408\n\t"
    "ds_read_b64_tr_b16 v[244:245], v13 offset:20480\n\t"
    "ds_read_b64_tr_b16 v[246:247], v13 offset:21504\n\t"
    "ds_read_b64_tr_b16 v[248:249], v13 offset:24576\n\t"
    "ds_read_b64_tr_b16 v[250:251], v13 offset:25600\n\t"
    "ds_read_b64_tr_b16 v[252:253], v13 offset:28672\n\t"
    "ds_read_b64_tr_b16 v[254:255], v13 offset:29696\n\t"
    "s_waitcnt lgkmcnt(15)\n\t"
    "v_lshlrev_b32 v40, 16, v44\n\t"
    "v_and_b32 v41, 0xffff0000, v44\n\t"
    "v_lshlrev_b32 v42, 16, v45\n\t"
    "v_and_b32 v43, 0xffff0000, v45\n\t"
    "v_lshlrev_b32 v44, 16, v46\n\t"
    "v_and_b32 v45, 0xffff0000, v46\n\t"
    "v_lshlrev_b32 v46, 16, v47\n\t"
    "v_and_b32 v47, 0xffff0000, v47\n\t"
    "v_lshlrev_b32 v48, 16, v52\n\t"
    "s_add_u32 m0, s76, 0x4400\n\t"
    "s_nop 0\n\t"
    "buffer_load_dwordx4 v72, s[44:47], 0 offen nt lds\n\t"
    "v_and_b32 v49, 0xffff0000, v52\n\t"
    "v_lshlrev_b32 v50, 16, v53\n\t"
    "v_and_b32 v51, 0xffff0000, v53\n\t"
    "v_lshlrev_b32 v52, 16, v54\n\t"
    "v_and_b32 v53, 0xffff0000, v54\n\t"
    "v_lshlrev_b32 v54, 16, v55\n\t"
    "v_and_b32 v55, 0xffff0000, v55\n\t"
    "s_waitcnt lgkmcnt(14)\n\t"
    "s_nop 0\n\t"
    "v_mfma_f32_32x32x16_bf16 v[40:55], v[224:227], v[88:91], v[40:55]\n\t"
    "ds_read_b128 v[108:111], v2 offset:512\n\t"
    "s_waitcnt lgkmcnt(13)\n\t"
    "v_mfma_f32_32x32x16_bf16 v[40:55], v[228:231], v[92:95], v[40:55]\n\t"
    "ds_read_b128 v[88:91], v1 offset:768\n\t"
    "s_add_u32 m0, s83, 0x10d00\n\t"
    "s_nop 0\n\t"
    "buffer_load_dwordx4 v73, s[40:43], 0 offen nt lds\n\t"
    "s_waitcnt lgkmcnt(12)\n\t"
    "v_mfma_f32_32x32x16_bf16 v[40:55], v[232:235], v[96:99], v[40:55]\n\t"
    "ds_read_b128 v[92:95], v2 offset:768\n\t"
    "s_waitcnt lgkmcnt(11)\n\t"
    "v_mfma_f32_32x32x16_bf16 v[40:55], v[236:239], v[100:103], v[40:55]\n\t"
    "s_waitcnt lgkmcnt(2)\n\t"
    "ds_read_b64_tr_b16 v[112:113], v14\n\t"
    "ds_read_b64_tr_b16 v[114:115], v14 offset:256\n\t"
    "v_mfma_f32_32x32x16_bf16 v[40:55], v[240:243], v[104:107], v[40:55]\n\t"
    "s_add_u32 m0, s76, 0x6600\n\t"
    "s_nop 0\n\t"
    "buffer_load_dwordx4 v74, s[44:47], 0 offen nt lds\n\t"
    "v_mfma_f32_32x32x16_bf16 v[40:55], v[244:247], v[108:111], v[40:55]\n\t"
    "s_waitcnt lgkmcnt(2)\n\t"
    "v_mfma_f32_32x32x16_bf16 v[40:55], v[248:251], v[88:91], v[40:55]\n\t"
    "ds_read_b64_tr_b16 v[116:117], v14 offset:1024\n\t"
    "ds_read_b64_tr_b16 v[118:119], v14 offset:1280\n\t"
    "v_mfma_f32_32x32x16_bf16 v[40:55], v[252:255], v[92:95], v[40:55]\n\t"
    "s_nop 11\n\t"
    "v_cvt_pk_bf16_f32 v28, v40, v41\n\t"
    "s_add_u32 m0, s83, 0x12d80\n\t"
    "s_nop 0\n\t"
    "buffer_load_dwordx4 v75, s[40:43], 0 offen nt lds\n\t"
    "v_cvt_pk_bf16_f32 v29, v42, v43\n\t"
    "v_cvt_pk_bf16_f32 v30, v44, v45\n\t"
    "v_cvt_pk_bf16_f32 v31, v46, v47\n\t"
    "ds_write_b128 v4, v[28:31]\n\t"
    "v_cvt_pk_bf16_f32 v28, v48, v49\n\t"
    "v_cvt_pk_bf16_f32 v29, v50, v51\n\t"
    "v_cvt_pk_bf16_f32 v30, v52, v53\n\t"
    "v_cvt_pk_bf16_f32 v31, v54, v55\n\t"
    "ds_write_b128 v4, v[28:31] offset:16\n\t"
    "v_add_f32 v32, v40, v41\n\t"
    "v_add_f32 v33, v42, v43\n\t"
    "v_add_f32 v34, v44, v45\n\t"
    "v_add_f32 v35, v46, v47\n\t"
    "v_add_f32 v32, v32, v48\n\t"
    "v_add_f32 v33, v33, v49\n\t"
    "s_waitcnt vmcnt(4)\n\t"
    "ds_read_b64_tr_b16 v[120:121], v14 offset:2048\n\t"
    "ds_read_b64_tr_b16 v[122:123], v14 offset:2304\n\t"
    "v_add_f32 v34, v34, v50\n\t"
    "v_add_f32 v35, v35, v51\n\t"
    "v_add_f32 v32, v32, v52\n\t"
    "v_add_f32 v33, v33, v53\n\t"
    "v_add_f32 v34, v34, v54\n\t"
    "v_add_f32 v35, v35, v55\n\t"
    "v_add_f32 v32, v32, v33\n\t"
    "v_add_f32 v34, v34, v35\n\t"
    "v_add_f32 v32, v32, v34\n\t"
    "v_mov_b32 v33, v32\n\t"
    "s_nop 1\n\t"
    "v_permlane32_swap_b32 v32, v33\n\t"
    "v_add_f32 v32, v32, v33\n\t"
    "v_mul_f32 v26, 0x3d000000, v32\n\t"
    "v_sub_f32 v40, v40, v26\n\t"
    "v_sub_f32 v41, v41, v26\n\t"
    "v_sub_f32 v42, v42, v26\n\t"
    "s_add_u32 m0, s85, 0\n\t"
    "s_mov_b32 s82, 0x6000\n\t"
    "buffer_load_dwordx4 v15, s[56:59], s82 offen lds\n\t"
    "v_sub_f32 v43, v43, v26\n\t"
    "v_sub_f32 v44, v44, v26\n\t"
    "v_sub_f32 v45, v45, v26\n\t"
    "v_sub_f32 v46, v46, v26\n\t"
    "v_sub_f32 v47, v47, v26\n\t"
    "v_sub_f32 v48, v48, v26\n\t"
    "v_sub_f32 v49, v49, v26\n\t"
    "v_sub_f32 v50, v50, v26\n\t"
    "v_sub_f32 v51, v51, v26\n\t"
    "v_sub_f32 v52, v52, v26\n\t"
    "v_sub_f32 v53, v53, v26\n\t"
    "v_sub_f32 v54, v54, v26\n\t"
    "v_sub_f32 v55, v55, v26\n\t"
    "v_mul_f32 v32, v40, v40\n\t"
    "v_mul_f32 v33, v41, v41\n\t"
    "v_fmac_f32 v32, v42, v42\n\t"
    "v_fmac_f32 v33, v43, v43\n\t"
    "v_fmac_f32 v32, v44, v44\n\t"
    "v_fmac_f32 v33, v45, v45\n\t"
    "v_fmac_f32 v32, v46, v46\n\t"
    "v_fmac_f32 v33, v47, v47\n\t"
    "v_fmac_f32 v32, v48, v48\n\t"
    "v_fmac_f32 v33, v49, v49\n\t"
    "v_fmac_f32 v32, v50, v50\n\t"
    "v_fmac_f32 v33, v51, v51\n\t"
    "v_fmac_f32 v32, v52, v52\n\t"
    "v_fmac_f32 v33, v53, v53\n\t"
    "v_fmac_f32 v32, v54, v54\n\t"
    "v_fmac_f32 v33, v55, v55\n\t"
    "v_add_f32 v27, v32, v33\n\t"
    "v_mov_b32 v32, v27\n\t"
    "s_add_u32 m0, s85, 0x2200\n\t"
    "s_mov_b32 s82, 0x8000\n\t"
    "buffer_load_dwordx4 v15, s[56:59], s82 offen lds\n\t"
    "v_permlane32_swap_b32 v27, v32\n\t"
    "v_add_f32 v27, v27, v32\n\t"
    "ds_write_b64 v11, v[26:27]\n\t"
    "s_waitcnt lgkmcnt(0)\n\t"
    "s_barrier\n\t"
    "ds_read_b128 v[28:31], v12\n\t"
    "ds_read_b128 v[32:35], v12 offset:16\n\t"
    "s_waitcnt lgkmcnt(1)\n\t"
    "v_add_f32 v36, v28, v30\n\t"
    "s_waitcnt lgkmcnt(0)\n\t"
    "v_add_f32 v37, v32, v34\n\t"
    "v_add_f32 v38, v29, v31\n\t"
    "v_add_f32 v39, v33, v35\n\t"
    "v_add_f32 v36, v36, v37\n\t"
    "v_add_f32 v37, v38, v39\n\t"
    "v_mul_f32 v36, 0x3e800000, v36\n\t"
    "v_sub_f32 v28, v28, v36\n\t"
    "s_add_u32 m0, s23, 0\n\t"
    "s_mov_b32 s82, 0xa000\n\t"
    "buffer_load_dwordx4 v15, s[56:59], s82 offen lds\n\t"
    "v_sub_f32 v30, v30, v36\n\t"
    "v_sub_f32 v32, v32, v36\n\t"
    "v_sub_f32 v34, v34, v36\n\t"
    "v_mul_f32 v38, v28, v28\n\t"
    "v_mul_f32 v39, v30, v30\n\t"
    "v_fmac_f32 v38, v32, v32\n\t"
    "v_fmac_f32 v39, v34, v34\n\t"
    "v_add_f32 v38, v38, v39\n\t"
    "v_fmamk_f32 v37, v38, 0x42000000, v37\n\t"
    "v_sub_f32 v38, v26, v36\n\t"
    "v_mul_f32 v37, 0x3c000000, v37\n\t"
    "v_add_f32 v37, 0x3727c5ac, v37\n\t"
    "v_rsq_f32 v37, v37\n\t"
    "s_nop 0\n\t"
    "v_mul_f32 v21, v37, v18\n\t"
    "v_mul_f32 v22, v38, v21\n\t"
    "v_fma_f32 v40, v40, v21, v22\n\t"
    "v_fma_f32 v41, v41, v21, v22\n\t"
    "v_fma_f32 v42, v42, v21, v22\n\t"
    "v_fma_f32 v43, v43, v21, v22\n\t"
    "v_fma_f32 v44, v44, v21, v22\n\t"
    "v_fma_f32 v45, v45, v21, v22\n\t"
    "v_fma_f32 v46, v46, v21, v22\n\t"
    "v_fma_f32 v47, v47, v21, v22\n\t"
    "v_mul_f32 v32, 0xbfb8aa3b, v40\n\t"
    "v_mul_f32 v33, 0xbfb8aa3b, v41\n\t"
    "v_mul_f32 v34, 0xbfb8aa3b, v42\n\t"
    "v_mul_f32 v35, 0xbfb8aa3b, v43\n\t"
    "v_mul_f32 v36, 0xbfb8aa3b, v44\n\t"
    "s_add_u32 m0, s23, 0x2080\n\t"
    "s_mov_b32 s82, 0xc000\n\t"
    "buffer_load_dwordx4 v15, s[56:59], s82 offen lds\n\t"
    "v_mul_f32 v37, 0xbfb8aa3b, v45\n\t"
    "v_mul_f32 v38, 0xbfb8aa3b, v46\n\t"
    "v_mul_f32 v39, 0xbfb8aa3b, v47\n\t"
    "v_exp_f32 v32, v32\n\t"
    "v_exp_f32 v33, v33\n\t"
    "v_exp_f32 v34, v34\n\t"
    "v_exp_f32 v35, v35\n\t"
    "v_exp_f32 v36, v36\n\t"
    "v_exp_f32 v37, v37\n\t"
    "v_exp_f32 v38, v38\n\t"
    "v_exp_f32 v39, v39\n\t"
    "s_add_u32 m0, s81, 0\n\t"
    "s_mov_b32 s82, 0xe000\n\t"
    "buffer_load_dwordx4 v15, s[56:59], s82 offen lds\n\t"
    "v_add_f32 v32, 1.0, v32\n\t"
    "v_add_f32 v33, 1.0, v33\n\t"
    "v_add_f32 v34, 1.0, v34\n\t"
    "v_add_f32 v35, 1.0, v35\n\t"
    "v_add_f32 v36, 1.0, v36\n\t"
    "v_add_f32 v37, 1.0, v37\n\t"
    "v_add_f32 v38, 1.0, v38\n\t"
    "v_add_f32 v39, 1.0, v39\n\t"
    "v_rcp_f32 v32, v32\n\t"
    "v_rcp_f32 v33, v33\n\t"
    "v_rcp_f32 v34, v34\n\t"
    "v_rcp_f32 v35, v35\n\t"
    "v_rcp_f32 v36, v36\n\t"
    "v_rcp_f32 v37, v37\n\t"
    "v_rcp_f32 v38, v38\n\t"
    "s_add_u32 m0, s81, 0x400\n\t"
    "s_mov_b32 s82, 0x10000\n\t"
    "buffer_load_dwordx4 v15, s[56:59], s82 offen lds\n\t"
    "v_rcp_f32 v39, v39\n\t"
    "v_mul_f32 v40, v40, v32\n\t"
    "v_mul_f32 v41, v41, v33\n\t"
    "v_mul_f32 v42, v42, v34\n\t"
    "v_mul_f32 v43, v43, v35\n\t"
    "v_mul_f32 v44, v44, v36\n\t"
    "v_mul_f32 v45, v45, v37\n\t"
    "v_mul_f32 v46, v46, v38\n\t"
    "v_mul_f32 v47, v47, v39\n\t"
    "v_cvt_pk_bf16_f32 v28, v40, v41\n\t"
    "v_cvt_pk_bf16_f32 v29, v42, v43\n\t"
    "v_cvt_pk_bf16_f32 v30, v44, v45\n\t"
    "v_cvt_pk_bf16_f32 v31, v46, v47\n\t"
    "ds_write_b128 v3, v[28:31]\n\t"
    "v_fma_f32 v48, v48, v21, v22\n\t"
    "v_fma_f32 v49, v49, v21, v22\n\t"
    "v_fma_f32 v50, v50, v21, v22\n\t"
    "v_fma_f32 v51, v51, v21, v22\n\t"
    "v_fma_f32 v52, v52, v21, v22\n\t"
    "v_fma_f32 v53, v53, v21, v22\n\t"
    "v_fma_f32 v54, v54, v21, v22\n\t"
    "v_fma_f32 v55, v55, v21, v22\n\t"
    "v_mul_f32 v32, 0xbfb8aa3b, v48\n\t"
    "v_mul_f32 v33, 0xbfb8aa3b, v49\n\t"
    "v_mul_f32 v34, 0xbfb8aa3b, v50\n\t"
    "v_mul_f32 v35, 0xbfb8aa3b, v51\n\t"
    "v_mul_f32 v36, 0xbfb8aa3b, v52\n\t"
    "v_mul_f32 v37, 0xbfb8aa3b, v53\n\t"
    "s_add_u32 m0, s81, 0x800\n\t"
    "s_mov_b32 s82, 0x12000\n\t"
    "buffer_load_dwordx4 v15, s[56:59], s82 offen lds\n\t"
    "v_mul_f32 v38, 0xbfb8aa3b, v54\n\t"
    "v_mul_f32 v39, 0xbfb8aa3b, v55\n\t"
    "v_exp_f32 v32, v32\n\t"
    "v_exp_f32 v33, v33\n\t"
    "v_exp_f32 v34, v34\n\t"
    "v_exp_f32 v35, v35\n\t"
    "v_exp_f32 v36, v36\n\t"
    "v_exp_f32 v37, v37\n\t"
    "v_exp_f32 v38, v38\n\t"
    "v_exp_f32 v39, v39\n\t"
    "v_add_f32 v32, 1.0, v32\n\t"
    "s_add_u32 m0, s84, 0\n\t"
    "s_mov_b32 s82, 0x14000\n\t"
    "buffer_load_dwordx4 v15, s[56:59], s82 offen lds\n\t"
    "v_add_f32 v33, 1.0, v33\n\t"
    "v_add_f32 v34, 1.0, v34\n\t"
    "v_add_f32 v35, 1.0, v35\n\t"
    "v_add_f32 v36, 1.0, v36\n\t"
    "v_add_f32 v37, 1.0, v37\n\t"
    "v_add_f32 v38, 1.0, v38\n\t"
    "v_add_f32 v39, 1.0, v39\n\t"
    "v_rcp_f32 v32, v32\n\t"
    "v_rcp_f32 v33, v33\n\t"
    "v_rcp_f32 v34, v34\n\t"
    "v_rcp_f32 v35, v35\n\t"
    "v_rcp_f32 v36, v36\n\t"
    "v_rcp_f32 v37, v37\n\t"
    "v_rcp_f32 v38, v38\n\t"
    "s_add_u32 m0, s84, 0x400\n\t"
    "s_mov_b32 s82, 0x16000\n\t"
    "buffer_load_dwordx4 v15, s[56:59], s82 offen lds\n\t"
    "v_rcp_f32 v39, v39\n\t"
    "v_mul_f32 v48, v48, v32\n\t"
    "v_mul_f32 v49, v49, v33\n\t"
    "v_mul_f32 v50, v50, v34\n\t"
    "v_mul_f32 v51, v51, v35\n\t"
    "v_mul_f32 v52, v52, v36\n\t"
    "v_mul_f32 v53, v53, v37\n\t"
    "v_mul_f32 v54, v54, v38\n\t"
    "v_mul_f32 v55, v55, v39\n\t"
    "v_cvt_pk_bf16_f32 v28, v48, v49\n\t"
    "v_cvt_pk_bf16_f32 v29, v50, v51\n\t"
    "v_cvt_pk_bf16_f32 v30, v52, v53\n\t"
    "v_cvt_pk_bf16_f32 v31, v54, v55\n\t"
    "ds_write_b128 v3, v[28:31] offset:16\n\t"
    "s_waitcnt vmcnt(9)\n\t"
    "s_waitcnt lgkmcnt(0)\n\t"
    "s_barrier\n\t"
    "s_add_u32 m0, s84, 0x800\n\t"
    "s_mov_b32 s82, 0x18000\n\t"
    "buffer_load_dwordx4 v15, s[56:59], s82 offen lds\n\t"
    "ds_read_b128 v[44:47], v4 offset:16640\n\t"
    "ds_read_b128 v[52:55], v4 offset:16656\n\t"
    "ds_read_b128 v[96:99], v1 offset:17408\n\t"
    "ds_read_b128 v[100:103], v2 offset:17408\n\t"
    "ds_read_b128 v[104:107], v1 offset:17664\n\t"
    "ds_read_b128 v[108:111], v2 offset:17664\n\t"
    "ds_read_b128 v[88:91], v1 offset:17920\n\t"
    "ds_read_b128 v[60:63], v8 offset:4160\n\t"
    "s_waitcnt lgkmcnt(7)\n\t"
    "v_lshlrev_b32 v40, 16, v44\n\t"
    "v_and_b32 v41, 0xffff0000, v44\n\t"
    "v_lshlrev_b32 v42, 16, v45\n\t"
    "v_and_b32 v43, 0xffff0000, v45\n\t"
    "v_lshlrev_b32 v44, 16, v46\n\t"
    "v_and_b32 v45, 0xffff0000, v46\n\t"
    "v_lshlrev_b32 v46, 16, v47\n\t"
    "v_and_b32 v47, 0xffff0000, v47\n\t"
    "s_waitcnt lgkmcnt(6)\n\t"
    "v_lshlrev_b32 v48, 16, v52\n\t"
    "v_and_b32 v49, 0xffff0000, v52\n\t"
    "v_lshlrev_b32 v50, 16, v53\n\t"
    "v_and_b32 v51, 0xffff0000, v53\n\t"
    "v_lshlrev_b32 v52, 16, v54\n\t"
    "v_and_b32 v53, 0xffff0000, v54\n\t"
    "v_lshlrev_b32 v54, 16, v55\n\t"
    "v_and_b32 v55, 0xffff0000, v55\n\t"
    "s_waitcnt lgkmcnt(4)\n\t"
    "s_nop 0\n\t"
    "v_mfma_f32_32x32x16_bf16 v[40:55], v[224:227], v[96:99], v[40:55]\n\t"
    "ds_read_b128 v[92:95], v2 offset:17920\n\t"
    "s_add_u32 m0, s84, 0xc00\n\t"
    "s_mov_b32 s82, 0x1a000\n\t"
    "buffer_load_dwordx4 v15, s[56:59], s82 offen lds\n\t"
    "v_mfma_f32_32x32x16_bf16 v[40:55], v[228:231], v[100:103], v[40:55]\n\t"
    "ds_read_b128 v[96:99], v1 offset:18176\n\t"
    "s_waitcnt lgkmcnt(2)\n\t"
    "v_lshlrev_b32 v56, 16, v60\n\t"
    "v_and_b32 v57, 0xffff0000, v60\n\t"
    "v_lshlrev_b32 v58, 16, v61\n\t"
    "v_and_b32 v59, 0xffff0000, v61\n\t"
    "v_lshlrev_b32 v60, 16, v62\n\t"
    "v_and_b32 v61, 0xffff0000, v62\n\t"
    "v_lshlrev_b32 v62, 16, v63\n\t"
    "v_and_b32 v63, 0xffff0000, v63\n\t"
    "v_mov_b32 v64, 0\n\t"
    "v_mov_b32 v65, 0\n\t"
    "v_mov_b32 v66, 0\n\t"
    "v_mov_b32 v67, 0\n\t"
    "v_mov_b32 v68, 0\n\t"
    "v_mov_b32 v69, 0\n\t"
    "v_mov_b32 v70, 0\n\t"
    "v_mov_b32 v71, 0\n\t"
    "v_mfma_f32_32x32x16_bf16 v[40:55], v[232:235], v[104:107], v[40:55]\n\t"
    "ds_read_b128 v[100:103], v2 offset:18176\n\t"
    "v_mfma_f32_32x32x16_bf16 v[40:55], v[236:239], v[108:111], v[40:55]\n\t"
    "ds_read_b128 v[104:107], v6\n\t"
    "s_waitcnt lgkmcnt(3)\n\t"
    "s_waitcnt vmcnt(10)\n\t"
    "ds_read_b64_tr_b16 v[124:125], v220\n\t"
    "ds_read_b64_tr_b16 v[126:127], v220 offset:256\n\t"
    "v_mfma_f32_32x32x16_bf16 v[40:55], v[240:243], v[88:91], v[40:55]\n\t"
    "ds_read_b128 v[108:111], v6 offset:64\n\t"
    "s_waitcnt vmcnt(9)\n\t"
    "ds_read_b64_tr_b16 v[128:129], v220 offset:8704\n\t"
    "ds_read_b64_tr_b16 v[130:131], v220 offset:8960\n\t"
    "v_mfma_f32_32x32x16_bf16 v[40:55], v[244:247], v[92:95], v[40:55]\n\t"
    "ds_read_b128 v[88:91], v6 offset:128\n\t"
    "s_waitcnt lgkmcnt(7)\n\t"
    "s_waitcnt vmcnt(8)\n\t"
    "ds_read_b64_tr_b16 v[132:133], v221\n\t"
    "ds_read_b64_tr_b16 v[134:135], v221 offset:256\n\t"
    "v_mfma_f32_32x32x16_bf16 v[40:55], v[248:251], v[96:99], v[40:55]\n\t"
    "ds_read_b128 v[92:95], v6 offset:192\n\t"
    "s_waitcnt vmcnt(7)\n\t"
    "ds_read_b64_tr_b16 v[136:137], v221 offset:8320\n\t"
    "ds_read_b64_tr_b16 v[138:139], v221 offset:8576\n\t"
    "v_mfma_f32_32x32x16_bf16 v[40:55], v[252:255], v[100:103], v[40:55]\n\t"
    "s_waitcnt lgkmcnt(9)\n\t"
    "s_add_u32 m0, s85, 0\n\t"
    "s_mov_b32 s82, 0x1c000\n\t"
    "buffer_load_dwordx4 v15, s[56:59], s82 offen lds\n\t"
    "ds_read_b128 v[96:99], v6 offset:272\n\t"
    "s_waitcnt lgkmcnt(9)\n\t"
    "s_waitcnt vmcnt(7)\n\t"
    "ds_read_b64_tr_b16 v[140:141], v14\n\t"
    "ds_read_b64_tr_b16 v[142:143], v14 offset:256\n\t"
    "v_mfma_f32_32x32x16_bf16 v[56:71], v[112:115], v[104:107], v[56:71]\n\t"
    "ds_read_b128 v[100:103], v6 offset:336\n\t"
    "s_waitcnt vmcnt(6)\n\t"
    "ds_read_b64_tr_b16 v[144:145], v14 offset:1024\n\t"
    "ds_read_b64_tr_b16 v[146:147], v14 offset:1280\n\t"
    "v_mfma_f32_32x32x16_bf16 v[56:71], v[116:119], v[108:111], v[56:71]\n\t"
    "ds_read_b128 v[104:107], v6 offset:400\n\t"
    "v_cvt_pk_bf16_f32 v28, v40, v41\n\t"
    "v_cvt_pk_bf16_f32 v29, v42, v43\n\t"
    "v_cvt_pk_bf16_f32 v30, v44, v45\n\t"
    "s_waitcnt lgkmcnt(9)\n\t"
    "s_waitcnt vmcnt(5)\n\t"
    "ds_read_b64_tr_b16 v[148:149], v14 offset:2048\n\t"
    "ds_read_b64_tr_b16 v[150:151], v14 offset:2304\n\t"
    "v_mfma_f32_32x32x16_bf16 v[56:71], v[120:123], v[88:91], v[56:71]\n\t"
    "ds_read_b128 v[108:111], v6 offset:464\n\t"
    "v_cvt_pk_bf16_f32 v31, v46, v47\n\t"
    "ds_write_b128 v4, v[28:31] offset:16640\n\t"
    "v_cvt_pk_bf16_f32 v28, v48, v49\n\t"
    "s_waitcnt vmcnt(4)\n\t"
    "ds_read_b64_tr_b16 v[152:153], v25\n\t"
    "ds_read_b64_tr_b16 v[154:155], v25 offset:256\n\t"
    "v_mfma_f32_32x32x16_bf16 v[56:71], v[124:127], v[92:95], v[56:71]\n\t"
    "s_waitcnt lgkmcnt(10)\n\t"
    "s_add_u32 m0, s81, 0\n\t"
    "s_mov_b32 s82, 0x1e000\n\t"
    "buffer_load_dwordx4 v15, s[56:59], s82 offen lds\n\t"
    "ds_read_b128 v[88:91], v6 offset:544\n\t"
    "v_cvt_pk_bf16_f32 v29, v50, v51\n\t"
    "v_cvt_pk_bf16_f32 v30, v52, v53\n\t"
    "v_cvt_pk_bf16_f32 v31, v54, v55\n\t"
    "s_waitcnt lgkmcnt(10)\n\t"
    "s_waitcnt vmcnt(4)\n\t"
    "ds_read_b64_tr_b16 v[156:157], v25 offset:1024\n\t"
    "ds_read_b64_tr_b16 v[158:159], v25 offset:1280\n\t"
    "v_mfma_f32_32x32x16_bf16 v[56:71], v[128:131], v[96:99], v[56:71]\n\t"
    "ds_read_b128 v[92:95], v6 offset:608\n\t"
    "ds_write_b128 v4, v[28:31] offset:16656\n\t"
    "v_add_f32 v32, v40, v41\n\t"
    "v_add_f32 v33, v42, v43\n\t"
    "s_waitcnt vmcnt(3)\n\t"
    "ds_read_b64_tr_b16 v[160:161], v25 offset:2048\n\t"
    "ds_read_b64_tr_b16 v[162:163], v25 offset:2304\n\t"
    "v_mfma_f32_32x32x16_bf16 v[56:71], v[132:135], v[100:103], v[56:71]\n\t"
    "ds_read_b128 v[96:99], v6 offset:672\n\t"
    "v_add_f32 v34, v44, v45\n\t"
    "v_add_f32 v35, v46, v47\n\t"
    "s_waitcnt lgkmcnt(15)\n\t"
    "s_add_u32 m0, s81, 0x400\n\t"
    "s_mov_b32 s82, 0x20000\n\t"
    "buffer_load_dwordx4 v15, s[56:59], s82 offen lds\n\t"
    "v_add_f32 v32, v32, v48\n\t"
    "s_waitcnt lgkmcnt(11)\n\t"
    "s_waitcnt vmcnt(3)\n\t"
    "ds_read_b64_tr_b16 v[164:165], v25 offset:3072\n\t"
    "ds_read_b64_tr_b16 v[166:167], v25 offset:3328\n\t"
    "v_mfma_f32_32x32x16_bf16 v[56:71], v[136:139], v[104:107], v[56:71]\n\t"
    "ds_read_b128 v[100:103], v6 offset:736\n\t"
    "v_add_f32 v33, v33, v49\n\t"
    "v_add_f32 v34, v34, v50\n\t"
    "v_add_f32 v35, v35, v51\n\t"
    "v_mfma_f32_32x32x16_bf16 v[56:71], v[140:143], v[108:111], v[56:71]\n\t"
    "ds_read_b128 v[104:107], v6 offset:4896\n\t"
    "v_add_f32 v32, v32, v52\n\t"
    "v_add_f32 v33, v33, v53\n\t"
    "v_add_f32 v34, v34, v54\n\t"
    "s_waitcnt lgkmcnt(8)\n\t"
    "s_add_u32 m0, s81, 0x800\n\t"
    "s_mov_b32 s82, 0x22000\n\t"
    "buffer_load_dwordx4 v15, s[56:59], s82 offen lds\n\t"
    "v_mfma_f32_32x32x16_bf16 v[56:71], v[144:147], v[88:91], v[56:71]\n\t"
    "ds_read_b128 v[108:111], v6 offset:4960\n\t"
    "v_add_f32 v35, v35, v55\n\t"
    "v_add_f32 v32, v32, v33\n\t"
    "v_add_f32 v34, v34, v35\n\t"
    "v_mfma_f32_32x32x16_bf16 v[56:71], v[148:151], v[92:95], v[56:71]\n\t"
    "ds_read_b128 v[88:91], v6 offset:5024\n\t"
    "v_add_f32 v32, v32, v34\n\t"
    "v_mov_b32 v33, v32\n\t"
    "s_nop 1\n\t"
    "v_permlane32_swap_b32 v32, v33\n\t"
    "s_waitcnt lgkmcnt(3)\n\t"
    "v_mfma_f32_32x32x16_bf16 v[56:71], v[152:155], v[96:99], v[56:71]\n\t"
    "s_waitcnt vmcnt(3)\n\t"
    "ds_read_b64_tr_b16 v[168:169], v220\n\t"
    "ds_read_b64_tr_b16 v[170:171], v220 offset:256\n\t"
    "ds_read_b128 v[92:95], v6 offset:5088\n\t"
    "s_add_u32 m0, s84, 0\n\t"
    "s_mov_b32 s82, 0x24000\n\t"
    "buffer_load_dwordx4 v15, s[56:59], s82 offen lds\n\t"
    "v_add_f32 v32, v32, v33\n\t"
    "v_mul_f32 v26, 0x3d000000, v32\n\t"
    "v_sub_f32 v40, v40, v26\n\t"
    "v_mfma_f32_32x32x16_bf16 v[56:71], v[156:159], v[100:103], v[56:71]\n\t"
    "ds_read_b128 v[96:99], v6 offset:5168\n\t"
    "v_sub_f32 v41, v41, v26\n\t"
    "v_sub_f32 v42, v42, v26\n\t"
    "v_sub_f32 v43, v43, v26\n\t"
    "s_waitcnt lgkmcnt(5)\n\t"
    "v_mfma_f32_32x32x16_bf16 v[56:71], v[160:163], v[104:107], v[56:71]\n\t"
    "ds_read_b128 v[100:103], v6 offset:5232\n\t"
    "v_sub_f32 v44, v44, v26\n\t"
    "v_sub_f32 v45, v45, v26\n\t"
    "v_sub_f32 v46, v46, v26\n\t"
    "s_add_u32 m0, s84, 0x400\n\t"
    "s_mov_b32 s82, 0x26000\n\t"
    "buffer_load_dwordx4 v15, s[56:59], s82 offen lds\n\t"
    "v_sub_f32 v47, v47, v26\n\t"
    "v_sub_f32 v48, v48, v26\n\t"
    "v_sub_f32 v49, v49, v26\n\t"
    "v_sub_f32 v50, v50, v26\n\t"
    "v_sub_f32 v51, v51, v26\n\t"
    "v_sub_f32 v52, v52, v26\n\t"
    "v_sub_f32 v53, v53, v26\n\t"
    "v_sub_f32 v54, v54, v26\n\t"
    "v_sub_f32 v55, v55, v26\n\t"
    "v_mul_f32 v32, v40, v40\n\t"
    "v_mul_f32 v33, v41, v41\n\t"
    "v_fmac_f32 v32, v42, v42\n\t"
    "v_fmac_f32 v33, v43, v43\n\t"
    "v_fmac_f32 v32, v44, v44\n\t"
    "v_fmac_f32 v33, v45, v45\n\t"
    "v_fmac_f32 v32, v46, v46\n\t"
    "v_fmac_f32 v33, v47, v47\n\t"
    "v_fmac_f32 v32, v48, v48\n\t"
    "v_fmac_f32 v33, v49, v49\n\t"
    "v_fmac_f32 v32, v50, v50\n\t"
    "v_fmac_f32 v33, v51, v51\n\t"
    "v_fmac_f32 v32, v52, v52\n\t"
    "v_fmac_f32 v33, v53, v53\n\t"
    "s_waitcnt vmcnt(4)\n\t"
    "ds_read_b64_tr_b16 v[172:173], v14\n\t"
    "ds_read_b64_tr_b16 v[174:175], v14 offset:256\n\t"
    "v_fmac_f32 v32, v54, v54\n\t"
    "v_fmac_f32 v33, v55, v55\n\t"
    "v_add_f32 v27, v32, v33\n\t"
    "v_mov_b32 v32, v27\n\t"
    "s_nop 1\n\t"
    "v_permlane32_swap_b32 v27, v32\n\t"
    "v_add_f32 v27, v27, v32\n\t"
    "ds_write_b64 v11, v[26:27] offset:2048\n\t"
    "s_waitcnt lgkmcnt(0)\n\t"
    "s_barrier\n\t"
    "s_waitcnt vmcnt(3)\n\t"
    "ds_read_b64_tr_b16 v[176:177], v14 offset:1024\n\t"
    "ds_read_b64_tr_b16 v[178:179], v14 offset:1280\n\t"
    "s_add_u32 m0, s76, 0x8800\n\t"
    "s_nop 0\n\t"
    "buffer_load_dwordx4 v76, s[44:47], 0 offen nt lds\n\t"
    "ds_read_b128 v[28:31], v12 offset:2048\n\t"
    "ds_read_b128 v[32:35], v12 offset:2064\n\t"
    "v_mfma_f32_32x32x16_bf16 v[56:71], v[164:167], v[108:111], v[56:71]\n\t"
    "s_waitcnt vmcnt(3)\n\t"
    "ds_read_b64_tr_b16 v[180:181], v14 offset:2048\n\t"
    "ds_read_b64_tr_b16 v[182:183], v14 offset:2304\n\t"
    "ds_read_b128 v[104:107], v6 offset:5296\n\t"
    "v_mfma_f32_32x32x16_bf16 v[56:71], v[168:171], v[88:91], v[56:71]\n\t"
    "s_waitcnt vmcnt(2)\n\t"
    "ds_read_b64_tr_b16 v[184:185], v25\n\t"
    "ds_read_b64_tr_b16 v[186:187], v25 offset:256\n\t"
    "ds_read_b128 v[108:111], v6 offset:5360\n\t"
    "s_waitcnt lgkmcnt(7)\n\t"
    "v_add_f32 v36, v28, v30\n\t"
    "s_waitcnt lgkmcnt(6)\n\t"
    "v_add_f32 v37, v32, v34\n\t"
    "v_add_f32 v38, v29, v31\n\t"
    "v_mfma_f32_32x32x16_bf16 v[56:71], v[172:175], v[92:95], v[56:71]\n\t"
    "s_add_u32 m0, s83, 0x14e00\n\t"
    "s_nop 0\n\t"
    "buffer_load_dwordx4 v77, s[40:43], 0 offen nt lds\n\t"
    "ds_read_b128 v[88:91], v6 offset:5440\n\t"
    "v_add_f32 v39, v33, v35\n\t"
    "v_add_f32 v36, v36, v37\n\t"
    "v_add_f32 v37, v38, v39\n\t"
    "v_mfma_f32_32x32x16_bf16 v[56:71], v[176:179], v[96:99], v[56:71]\n\t"
    "ds_read_b128 v[92:95], v6 offset:5504\n\t"
    "v_mul_f32 v36, 0x3e800000, v36\n\t"
    "v_sub_f32 v28, v28, v36\n\t"
    "v_sub_f32 v30, v30, v36\n\t"
    "s_waitcnt lgkmcnt(6)\n\t"
    "v_mfma_f32_32x32x16_bf16 v[56:71], v[180:183], v[100:103], v[56:71]\n\t"
    "ds_read_b128 v[96:99], v6 offset:5568\n\t"
    "v_sub_f32 v32, v32, v36\n\t"
    "v_sub_f32 v34, v34, v36\n\t"
    "s_add_u32 m0, s76, 0xaa00\n\t"
    "s_nop 0\n\t"
    "buffer_load_dwordx4 v78, s[44:47], 0 offen nt lds\n\t"
    "v_mul_f32 v38, v28, v28\n\t"
    "s_waitcnt lgkmcnt(3)\n\t"
    "v_mfma_f32_32x32x16_bf16 v[56:71], v[184:187], v[104:107], v[56:71]\n\t"
    "ds_read_b128 v[100:103], v6 offset:5632\n\t"
    "v_mul_f32 v39, v30, v30\n\t"
    "v_fmac_f32 v38, v32, v32\n\t"
    "v_fmac_f32 v39, v34, v34\n\t"
    "v_add_f32 v38, v38, v39\n\t"
    "v_fmamk_f32 v37, v38, 0x42000000, v37\n\t"
    "v_sub_f32 v38, v26, v36\n\t"
    "v_mul_f32 v37, 0x3c000000, v37\n\t"
    "v_add_f32 v37, 0x3727c5ac, v37\n\t"
    "v_rsq_f32 v37, v37\n\t"
    "s_nop 0\n\t"
    "v_mul_f32 v21, v37, v19\n\t"
    "v_mul_f32 v22, v38, v21\n\t"
    "v_fma_f32 v40, v40, v21, v22\n\t"
    "v_fma_f32 v41, v41, v21, v22\n\t"
    "v_fma_f32 v42, v42, v21, v22\n\t"
    "v_fma_f32 v43, v43, v21, v22\n\t"
    "v_fma_f32 v44, v44, v21, v22\n\t"
    "v_fma_f32 v45, v45, v21, v22\n\t"
    "s_add_u32 m0, s83, 0x16e80\n\t"
    "s_nop 0\n\t"
    "buffer_load_dwordx4 v79, s[40:43], 0 offen nt lds\n\t"
    "v_fma_f32 v46, v46, v21, v22\n\t"
    "v_fma_f32 v47, v47, v21, v22\n\t"
    "v_mul_f32 v32, 0xbfb8aa3b, v40\n\t"
    "v_mul_f32 v33, 0xbfb8aa3b, v41\n\t"
    "v_mul_f32 v34, 0xbfb8aa3b, v42\n\t"
    "v_mul_f32 v35, 0xbfb8aa3b, v43\n\t"
    "v_mul_f32 v36, 0xbfb8aa3b, v44\n\t"
    "v_mul_f32 v37, 0xbfb8aa3b, v45\n\t"
    "v_mul_f32 v38, 0xbfb8aa3b, v46\n\t"
    "v_mul_f32 v39, 0xbfb8aa3b, v47\n\t"
    "v_exp_f32 v32, v32\n\t"
    "s_waitcnt vmcnt(4)\n\t"
    "ds_read_b64_tr_b16 v[188:189], v25 offset:1024\n\t"
    "ds_read_b64_tr_b16 v[190:191], v25 offset:1280\n\t"
    "v_exp_f32 v33, v33\n\t"
    "v_exp_f32 v34, v34\n\t"
    "v_exp_f32 v35, v35\n\t"
    "v_exp_f32 v36, v36\n\t"
    "v_exp_f32 v37, v37\n\t"
    "s_add_u32 m0, s84, 0x800\n\t"
    "s_mov_b32 s82, 0x28000\n\t"
    "buffer_load_dwordx4 v15, s[56:59], s82 offen lds\n\t"
    "v_exp_f32 v38, v38\n\t"
    "v_exp_f32 v39, v39\n\t"
    "v_add_f32 v32, 1.0, v32\n\t"
    "v_add_f32 v33, 1.0, v33\n\t"
    "v_add_f32 v34, 1.0, v34\n\t"
    "v_add_f32 v35, 1.0, v35\n\t"
    "v_add_f32 v36, 1.0, v36\n\t"
    "v_add_f32 v37, 1.0, v37\n\t"
    "v_add_f32 v38, 1.0, v38\n\t"
    "v_add_f32 v39, 1.0, v39\n\t"
    "v_rcp_f32 v32, v32\n\t"
    "v_rcp_f32 v33, v33\n\t"
    "v_rcp_f32 v34, v34\n\t"
    "v_rcp_f32 v35, v35\n\t"
    "v_rcp_f32 v36, v36\n\t"
    "s_add_u32 m0, s84, 0xc00\n\t"
    "s_mov_b32 s82, 0x2a000\n\t"
    "buffer_load_dwordx4 v15, s[56:59], s82 offen lds\n\t"
    "v_rcp_f32 v37, v37\n\t"
    "v_rcp_f32 v38, v38\n\t"
    "v_rcp_f32 v39, v39\n\t"
    "v_mul_f32 v40, v40, v32\n\t"
    "v_mul_f32 v41, v41, v33\n\t"
    "v_mul_f32 v42, v42, v34\n\t"
    "v_mul_f32 v43, v43, v35\n\t"
    "v_mul_f32 v44, v44, v36\n\t"
    "v_mul_f32 v45, v45, v37\n\t"
    "v_mul_f32 v46, v46, v38\n\t"
    "v_mul_f32 v47, v47, v39\n\t"
    "v_cvt_pk_bf16_f32 v28, v40, v41\n\t"
    "v_cvt_pk_bf16_f32 v29, v42, v43\n\t"
    "v_cvt_pk_bf16_f32 v30, v44, v45\n\t"
    "v_cvt_pk_bf16_f32 v31, v46, v47\n\t"
    "ds_write_b128 v3, v[28:31] offset:17408\n\t"
    "v_fma_f32 v48, v48, v21, v22\n\t"
    "v_fma_f32 v49, v49, v21, v22\n\t"
    "v_fma_f32 v50, v50, v21, v22\n\t"
    "v_fma_f32 v51, v51, v21, v22\n\t"
    "v_fma_f32 v52, v52, v21, v22\n\t"
    "v_fma_f32 v53, v53, v21, v22\n\t"
    "v_fma_f32 v54, v54, v21, v22\n\t"
    "s_add_u32 m0, s81, 0\n\t"
    "s_mov_b32 s82, 0x2c000\n\t"
    "buffer_load_dwordx4 v15, s[56:59], s82 offen lds\n\t"
    "v_fma_f32 v55, v55, v21, v22\n\t"
    "v_mul_f32 v32, 0xbfb8aa3b, v48\n\t"
    "v_mul_f32 v33, 0xbfb8aa3b, v49\n\t"
    "v_mul_f32 v34, 0xbfb8aa3b, v50\n\t"
    "v_mul_f32 v35, 0xbfb8aa3b, v51\n\t"
    "v_mul_f32 v36, 0xbfb8aa3b, v52\n\t"
    "v_mul_f32 v37, 0xbfb8aa3b, v53\n\t"
    "v_mul_f32 v38, 0xbfb8aa3b, v54\n\t"
    "v_mul_f32 v39, 0xbfb8aa3b, v55\n\t"
    "v_exp_f32 v32, v32\n\t"
    "v_exp_f32 v33, v33\n\t"
    "v_exp_f32 v34, v34\n\t"
    "v_exp_f32 v35, v35\n\t"
    "v_exp_f32 v36, v36\n\t"
    "v_exp_f32 v37, v37\n\t"
    "v_exp_f32 v38, v38\n\t"
    "s_add_u32 m0, s81, 0x400\n\t"
    "s_mov_b32 s82, 0x2e000\n\t"
    "buffer_load_dwordx4 v15, s[56:59], s82 offen lds\n\t"
    "v_exp_f32 v39, v39\n\t"
    "v_add_f32 v32, 1.0, v32\n\t"
    "v_add_f32 v33, 1.0, v33\n\t"
    "v_add_f32 v34, 1.0, v34\n\t"
    "v_add_f32 v35, 1.0, v35\n\t"
    "v_add_f32 v36, 1.0, v36\n\t"
    "v_add_f32 v37, 1.0, v37\n\t"
    "v_add_f32 v38, 1.0, v38\n\t"
    "v_add_f32 v39, 1.0, v39\n\t"
    "v_rcp_f32 v32, v32\n\t"
    "v_rcp_f32 v33, v33\n\t"
    "v_rcp_f32 v34, v34\n\t"
    "v_rcp_f32 v35, v35\n\t"
    "v_rcp_f32 v36, v36\n\t"
    "v_rcp_f32 v37, v37\n\t"
    "s_waitcnt vmcnt(3)\n\t"
    "ds_read_b64_tr_b16 v[192:193], v25 offset:2048\n\t"
    "ds_read_b64_tr_b16 v[194:195], v25 offset:2304\n\t"
    "s_add_u32 m0, s81, 0x800\n\t"
    "s_mov_b32 s82, 0x30000\n\t"
    "buffer_load_dwordx4 v15, s[56:59], s82 offen lds\n\t"
    "v_rcp_f32 v38, v38\n\t"
    "v_rcp_f32 v39, v39\n\t"
    "v_mul_f32 v48, v48, v32\n\t"
    "v_mul_f32 v49, v49, v33\n\t"
    "v_mul_f32 v50, v50, v34\n\t"
    "v_mul_f32 v51, v51, v35\n\t"
    "s_waitcnt vmcnt(3)\n\t"
    "ds_read_b64_tr_b16 v[196:197], v25 offset:3072\n\t"
    "ds_read_b64_tr_b16 v[198:199], v25 offset:3328\n\t"
    "v_mul_f32 v52, v52, v36\n\t"
    "v_mul_f32 v53, v53, v37\n\t"
    "v_mul_f32 v54, v54, v38\n\t"
    "v_mul_f32 v55, v55, v39\n\t"
    "v_cvt_pk_bf16_f32 v28, v48, v49\n\t"
    "v_cvt_pk_bf16_f32 v29, v50, v51\n\t"
    "v_cvt_pk_bf16_f32 v30, v52, v53\n\t"
    "v_cvt_pk_bf16_f32 v31, v54, v55\n\t"
    "ds_write_b128 v3, v[28:31] offset:17424\n\t"
    "s_waitcnt lgkmcnt(0)\n\t"
    "s_barrier\n\t"
    "s_waitcnt vmcnt(2)\n\t"
    "ds_read_b64_tr_b16 v[200:201], v14\n\t"
    "ds_read_b64_tr_b16 v[202:203], v14 offset:256\n\t"
    "s_add_u32 m0, s84, 0\n\t"
    "s_mov_b32 s82, 0x32000\n\t"
    "buffer_load_dwordx4 v15, s[56:59], s82 offen lds\n\t"
    "v_mfma_f32_32x32x16_bf16 v[56:71], v[188:191], v[108:111], v[56:71]\n\t"
    "ds_read_b128 v[104:107], v1 offset:34816\n\t"
    "ds_read_b128 v[44:47], v4 offset:33280\n\t"
    "ds_read_b128 v[52:55], v4 offset:33296\n\t"
    "ds_read_b128 v[76:79], v8 offset:13520\n\t"
    "s_waitcnt vmcnt(2)\n\t"
    "ds_read_b64_tr_b16 v[204:205], v14 offset:1024\n\t"
    "ds_read_b64_tr_b16 v[206:207], v14 offset:1280\n\t"
    "v_mfma_f32_32x32x16_bf16 v[56:71], v[192:195], v[88:91], v[56:71]\n\t"
    "ds_read_b128 v[108:111], v2 offset:34816\n\t"
    "v_mfma_f32_32x32x16_bf16 v[56:71], v[196:199], v[92:95], v[56:71]\n\t"
    "ds_read_b128 v[88:91], v1 offset:35072\n\t"
    "s_add_u32 m0, s84, 0x400\n\t"
    "s_mov_b32 s82, 0x34000\n\t"
    "buffer_load_dwordx4 v15, s[56:59], s82 offen lds\n\t"
    "s_waitcnt lgkmcnt(6)\n\t"
    "v_lshlrev_b32 v40, 16, v44\n\t"
    "v_and_b32 v41, 0xffff0000, v44\n\t"
    "v_lshlrev_b32 v42, 16, v45\n\t"
    "v_and_b32 v43, 0xffff0000, v45\n\t"
    "v_lshlrev_b32 v44, 16, v46\n\t"
    "v_and_b32 v45, 0xffff0000, v46\n\t"
    "v_lshlrev_b32 v46, 16, v47\n\t"
    "v_and_b32 v47, 0xffff0000, v47\n\t"
    "s_waitcnt lgkmcnt(5)\n\t"
    "v_lshlrev_b32 v48, 16, v52\n\t"
    "v_and_b32 v49, 0xffff0000, v52\n\t"
    "v_lshlrev_b32 v50, 16, v53\n\t"
    "v_and_b32 v51, 0xffff0000, v53\n\t"
    "v_lshlrev_b32 v52, 16, v54\n\t"
    "v_and_b32 v53, 0xffff0000, v54\n\t"
    "v_lshlrev_b32 v54, 16, v55\n\t"
    "v_and_b32 v55, 0xffff0000, v55\n\t"
    "v_mfma_f32_32x32x16_bf16 v[56:71], v[200:203], v[96:99], v[56:71]\n\t"
    "ds_read_b128 v[92:95], v2 offset:35072\n\t"
    "s_waitcnt lgkmcnt(3)\n\t"
    "v_mfma_f32_32x32x16_bf16 v[56:71], v[204:207], v[100:103], v[56:71]\n\t"
    "s_add_u32 m0, s84, 0x800\n\t"
    "s_mov_b32 s82, 0x36000\n\t"
    "buffer_load_dwordx4 v15, s[56:59], s82 offen lds\n\t"
    "ds_read_b128 v[96:99], v1 offset:35328\n\t"
    "s_waitcnt lgkmcnt(3)\n\t"
    "v_mfma_f32_32x32x16_bf16 v[40:55], v[224:227], v[104:107], v[40:55]\n\t"
    "ds_read_b128 v[100:103], v2 offset:35328\n\t"
    "v_mfma_f32_32x32x16_bf16 v[40:55], v[228:231], v[108:111], v[40:55]\n\t"
    "ds_read_b128 v[104:107], v1 offset:35584\n\t"
    "v_lshlrev_b32 v72, 16, v76\n\t"
    "v_and_b32 v73, 0xffff0000, v76\n\t"
    "v_lshlrev_b32 v74, 16, v77\n\t"
    "v_and_b32 v75, 0xffff0000, v77\n\t"
    "v_lshlrev_b32 v76, 16, v78\n\t"
    "v_and_b32 v77, 0xffff0000, v78\n\t"
    "v_lshlrev_b32 v78, 16, v79\n\t"
    "v_and_b32 v79, 0xffff0000, v79\n\t"
    "v_mov_b32 v80, 0\n\t"
    "v_mov_b32 v81, 0\n\t"
    "v_mov_b32 v82, 0\n\t"
    "v_mov_b32 v83, 0\n\t"
    "v_mov_b32 v84, 0\n\t"
    "v_mov_b32 v85, 0\n\t"
    "v_mov_b32 v86, 0\n\t"
    "v_mov_b32 v87, 0\n\t"
    "s_waitcnt lgkmcnt(3)\n\t"
    "v_mfma_f32_32x32x16_bf16 v[40:55], v[232:235], v[88:91], v[40:55]\n\t"
    "ds_read_b128 v[108:111], v2 offset:35584\n\t"
    "v_mfma_f32_32x32x16_bf16 v[40:55], v[236:239], v[92:95], v[40:55]\n\t"
    "ds_read_b128 v[88:91], v6 offset:9792\n\t"
    "s_waitcnt lgkmcnt(3)\n\t"
    "v_mfma_f32_32x32x16_bf16 v[40:55], v[240:243], v[96:99], v[40:55]\n\t"
    "ds_read_b128 v[92:95], v6 offset:9856\n\t"
    "v_mfma_f32_32x32x16_bf16 v[40:55], v[244:247], v[100:103], v[40:55]\n\t"
    "ds_read_b128 v[96:99], v6 offset:9920\n\t"
    "s_waitcnt lgkmcnt(3)\n\t"
    "v_mfma_f32_32x32x16_bf16 v[40:55], v[248:251], v[104:107], v[40:55]\n\t"
    "ds_read_b128 v[100:103], v6 offset:9984\n\t"
    "v_mfma_f32_32x32x16_bf16 v[40:55], v[252:255], v[108:111], v[40:55]\n\t"
    "ds_read_b128 v[104:107], v6 offset:10064\n\t"
    "s_add_u32 m0, s84, 0xc00\n\t"
    "s_mov_b32 s82, 0x38000\n\t"
    "buffer_load_dwordx4 v15, s[56:59], s82 offen lds\n\t"
    "s_waitcnt lgkmcnt(3)\n\t"
    "v_mfma_f32_32x32x16_bf16 v[72:87], v[112:115], v[88:91], v[72:87]\n\t"
    "ds_read_b128 v[108:111], v6 offset:10128\n\t"
    "s_waitcnt vmcnt(4)\n\t"
    "ds_read_b64_tr_b16 v[208:209], v14 offset:2048\n\t"
    "ds_read_b64_tr_b16 v[210:211], v14 offset:2304\n\t"
    "v_mfma_f32_32x32x16_bf16 v[72:87], v[116:119], v[92:95], v[72:87]\n\t"
    "ds_read_b128 v[88:91], v6 offset:10192\n\t"
    "v_cvt_pk_bf16_f32 v28, v40, v41\n\t"
    "v_cvt_pk_bf16_f32 v29, v42, v43\n\t"
    "v_cvt_pk_bf16_f32 v30, v44, v45\n\t"
    "s_add_u32 m0, s81, 0\n\t"
    "s_mov_b32 s82, 0x3a000\n\t"
    "buffer_load_dwordx4 v15, s[56:59], s82 offen lds\n\t"
    "v_cvt_pk_bf16_f32 v31, v46, v47\n\t"
    "s_waitcnt lgkmcnt(5)\n\t"
    "s_waitcnt vmcnt(4)\n\t"
    "ds_read_b64_tr_b16 v[212:213], v25\n\t"
    "ds_read_b64_tr_b16 v[214:215], v25 offset:256\n\t"
    "v_mfma_f32_32x32x16_bf16 v[72:87], v[120:123], v[96:99], v[72:87]\n\t"
    "ds_read_b128 v[92:95], v6 offset:10256\n\t"
    "ds_write_b128 v4, v[28:31] offset:33280\n\t"
    "v_cvt_pk_bf16_f32 v28, v48, v49\n\t"
    "v_cvt_pk_bf16_f32 v29, v50, v51\n\t"
    "v_cvt_pk_bf16_f32 v30, v52, v53\n\t"
    "s_waitcnt vmcnt(3)\n\t"
    "ds_read_b64_tr_b16 v[216:217], v25 offset:1024\n\t"
    "ds_read_b64_tr_b16 v[218:219], v25 offset:1280\n\t"
    "v_mfma_f32_32x32x16_bf16 v[72:87], v[124:127], v[100:103], v[72:87]\n\t"
    "ds_read_b128 v[96:99], v6 offset:10336\n\t"
    "v_cvt_pk_bf16_f32 v31, v54, v55\n\t"
    "ds_write_b128 v4, v[28:31] offset:33296\n\t"
    "v_add_f32 v32, v40, v41\n\t"
    "v_add_f32 v33, v42, v43\n\t"
    "s_waitcnt lgkmcnt(11)\n\t"
    "s_waitcnt vmcnt(2)\n\t"
    "ds_read_b64_tr_b16 v[220:221], v25 offset:2048\n\t"
    "ds_read_b64_tr_b16 v[222:223], v25 offset:2304\n\t"
    "s_add_u32 m0, s81, 0x400\n\t"
    "s_mov_b32 s82, 0x3c000\n\t"
    "buffer_load_dwordx4 v15, s[56:59], s82 offen lds\n\t"
    "v_mfma_f32_32x32x16_bf16 v[72:87], v[128:131], v[104:107], v[72:87]\n\t"
    "ds_read_b128 v[100:103], v6 offset:10400\n\t"
    "v_add_f32 v34, v44, v45\n\t"
    "v_add_f32 v35, v46, v47\n\t"
    "v_add_f32 v32, v32, v48\n\t"
    "v_add_f32 v33, v33, v49\n\t"
    "v_mfma_f32_32x32x16_bf16 v[72:87], v[132:135], v[108:111], v[72:87]\n\t"
    "ds_read_b128 v[104:107], v6 offset:10464\n\t"
    "v_add_f32 v34, v34, v50\n\t"
    "v_add_f32 v35, v35, v51\n\t"
    "v_add_f32 v32, v32, v52\n\t"
    "v_add_f32 v33, v33, v53\n\t"
    "s_waitcnt lgkmcnt(9)\n\t"
    "v_mfma_f32_32x32x16_bf16 v[72:87], v[136:139], v[88:91], v[72:87]\n\t"
    "ds_read_b128 v[108:111], v6 offset:10528\n\t"
    "s_add_u32 m0, s81, 0x800\n\t"
    "s_mov_b32 s82, 0x3e000\n\t"
    "buffer_load_dwordx4 v15, s[56:59], s82 offen lds\n\t"
    "v_add_f32 v34, v34, v54\n\t"
    "v_add_f32 v35, v35, v55\n\t"
    "v_add_f32 v32, v32, v33\n\t"
    "v_add_f32 v34, v34, v35\n\t"
    "v_mfma_f32_32x32x16_bf16 v[72:87], v[140:143], v[92:95], v[72:87]\n\t"
    "ds_read_b128 v[88:91], v6 offset:14688\n\t"
    "v_add_f32 v32, v32, v34\n\t"
    "v_mov_b32 v33, v32\n\t"
    "s_nop 1\n\t"
    "v_permlane32_swap_b32 v32, v33\n\t"
    "v_add_f32 v32, v32, v33\n\t"
    "s_waitcnt lgkmcnt(3)\n\t"
    "v_mfma_f32_32x32x16_bf16 v[72:87], v[144:147], v[96:99], v[72:87]\n\t"
    "ds_read_b128 v[92:95], v6 offset:14752\n\t"
    "v_mul_f32 v26, 0x3d000000, v32\n\t"
    "v_sub_f32 v40, v40, v26\n\t"
    "v_sub_f32 v41, v41, v26\n\t"
    "s_add_u32 m0, s84, 0\n\t"
    "s_mov_b32 s82, 0x40000\n\t"
    "buffer_load_dwordx4 v15, s[56:59], s82 offen lds\n\t"
    "v_sub_f32 v42, v42, v26\n\t"
    "v_mfma_f32_32x32x16_bf16 v[72:87], v[148:151], v[100:103], v[72:87]\n\t"
    "ds_read_b128 v[96:99], v6 offset:14816\n\t"
    "v_sub_f32 v43, v43, v26\n\t"
    "v_sub_f32 v44, v44, v26\n\t"
    "v_sub_f32 v45, v45, v26\n\t"
    "v_sub_f32 v46, v46, v26\n\t"
    "s_waitcnt lgkmcnt(3)\n\t"
    "v_mfma_f32_32x32x16_bf16 v[72:87], v[152:155], v[104:107], v[72:87]\n\t"
    "ds_read_b128 v[100:103], v6 offset:14880\n\t"
    "s_waitcnt vmcnt(4)\n\t"
    "ds_read_b64_tr_b16 v[224:225], v25 offset:3072\n\t"
    "ds_read_b64_tr_b16 v[226:227], v25 offset:3328\n\t"
    "v_sub_f32 v47, v47, v26\n\t"
    "v_sub_f32 v48, v48, v26\n\t"
    "v_sub_f32 v49, v49, v26\n\t"
    "v_sub_f32 v50, v50, v26\n\t"
    "v_mfma_f32_32x32x16_bf16 v[72:87], v[156:159], v[108:111], v[72:87]\n\t"
    "ds_read_b128 v[104:107], v6 offset:14960\n\t"
    "v_sub_f32 v51, v51, v26\n\t"
    "v_sub_f32 v52, v52, v26\n\t"
    "v_sub_f32 v53, v53, v26\n\t"
    "v_sub_f32 v54, v54, v26\n\t"
    "s_waitcnt lgkmcnt(5)\n\t"
    "v_mfma_f32_32x32x16_bf16 v[72:87], v[160:163], v[88:91], v[72:87]\n\t"
    "ds_read_b128 v[108:111], v6 offset:15024\n\t"
    "v_sub_f32 v55, v55, v26\n\t"
    "v_mul_f32 v32, v40, v40\n\t"
    "v_mul_f32 v33, v41, v41\n\t"
    "s_waitcnt vmcnt(3)\n\t"
    "ds_read_b64_tr_b16 v[228:229], v14\n\t"
    "ds_read_b64_tr_b16 v[230:231], v14 offset:256\n\t"
    "v_fmac_f32 v32, v42, v42\n\t"
    "v_mfma_f32_32x32x16_bf16 v[72:87], v[164:167], v[92:95], v[72:87]\n\t"
    "ds_read_b128 v[88:91], v6 offset:15088\n\t"
    "v_fmac_f32 v33, v43, v43\n\t"
    "v_fmac_f32 v32, v44, v44\n\t"
    "v_fmac_f32 v33, v45, v45\n\t"
    "v_fmac_f32 v32, v46, v46\n\t"
    "s_waitcnt lgkmcnt(7)\n\t"
    "v_mfma_f32_32x32x16_bf16 v[72:87], v[168:171], v[96:99], v[72:87]\n\t"
    "ds_read_b128 v[92:95], v6 offset:15152\n\t"
    "v_fmac_f32 v33, v47, v47\n\t"
    "v_fmac_f32 v32, v48, v48\n\t"
    "v_fmac_f32 v33, v49, v49\n\t"
    "v_fmac_f32 v32, v50, v50\n\t"
    "s_waitcnt vmcnt(2)\n\t"
    "ds_read_b64_tr_b16 v[232:233], v14 offset:1024\n\t"
    "ds_read_b64_tr_b16 v[234:235], v14 offset:1280\n\t"
    "v_mfma_f32_32x32x16_bf16 v[72:87], v[172:175], v[100:103], v[72:87]\n\t"
    "ds_read_b128 v[96:99], v6 offset:9792\n\t"
    "v_fmac_f32 v33, v51, v51\n\t"
    "v_fmac_f32 v32, v52, v52\n\t"
    "v_fmac_f32 v33, v53, v53\n\t"
    "v_fmac_f32 v32, v54, v54\n\t"
    "s_waitcnt lgkmcnt(7)\n\t"
    "s_waitcnt lgkmcnt(5)\n\t"
    "s_add_u32 m0, s81, 0\n\t"
    "s_mov_b32 s82, 0x42000\n\t"
    "buffer_load_dwordx4 v15, s[56:59], s82 offen lds\n\t"
    "v_mfma_f32_32x32x16_bf16 v[72:87], v[176:179], v[104:107], v[72:87]\n\t"
    "ds_read_b128 v[100:103], v6 offset:9856\n\t"
    "v_fmac_f32 v33, v55, v55\n\t"
    "v_add_f32 v27, v32, v33\n\t"
    "v_mov_b32 v32, v27\n\t"
    "s_nop 1\n\t"
    "v_permlane32_swap_b32 v27, v32\n\t"
    "v_mfma_f32_32x32x16_bf16 v[72:87], v[180:183], v[108:111], v[72:87]\n\t"
    "ds_read_b128 v[104:107], v6 offset:9920\n\t"
    "s_waitcnt vmcnt(2)\n\t"
    "ds_read_b64_tr_b16 v[236:237], v14 offset:2048\n\t"
    "ds_read_b64_tr_b16 v[238:239], v14 offset:2304\n\t"
    "v_add_f32 v27, v27, v32\n\t"
    "ds_write_b64 v11, v[26:27]\n\t"
    "s_waitcnt lgkmcnt(8)\n\t"
    "v_mfma_f32_32x32x16_bf16 v[72:87], v[184:187], v[88:91], v[72:87]\n\t"
    "ds_read_b128 v[108:111], v6 offset:9984\n\t"
    "s_waitcnt lgkmcnt(7)\n\t"
    "s_add_u32 m0, s81, 0x400\n\t"
    "s_mov_b32 s82, 0x44000\n\t"
    "buffer_load_dwordx4 v15, s[56:59], s82 offen lds\n\t"
    "v_mfma_f32_32x32x16_bf16 v[72:87], v[188:191], v[92:95], v[72:87]\n\t"
    "ds_read_b128 v[88:91], v6 offset:15232\n\t"
    "s_waitcnt lgkmcnt(6)\n\t"
    "s_waitcnt vmcnt(2)\n\t"
    "ds_read_b64_tr_b16 v[240:241], v25\n\t"
    "ds_read_b64_tr_b16 v[242:243], v25 offset:256\n\t"
    "v_mfma_f32_32x32x16_bf16 v[56:71], v[208:211], v[96:99], v[56:71]\n\t"
    "ds_read_b128 v[92:95], v6 offset:15296\n\t"
    "s_waitcnt lgkmcnt(5)\n\t"
    "s_barrier\n\t"
    "s_add_u32 m0, s81, 0x800\n\t"
    "s_mov_b32 s82, 0x46000\n\t"
    "buffer_load_dwordx4 v15, s[56:59], s82 offen lds\n\t"
    "ds_read_b128 v[28:31], v12\n\t"
    "ds_read_b128 v[32:35], v12 offset:16\n\t"
    "v_mfma_f32_32x32x16_bf16 v[56:71], v[212:215], v[100:103], v[56:71]\n\t"
    "ds_read_b128 v[96:99], v6 offset:15360\n\t"
    "s_waitcnt lgkmcnt(7)\n\t"
    "v_mfma_f32_32x32x16_bf16 v[56:71], v[216:219], v[104:107], v[56:71]\n\t"
    "ds_read_b128 v[100:103], v6 offset:15424\n\t"
    "s_waitcnt lgkmcnt(3)\n\t"
    "v_add_f32 v36, v28, v30\n\t"
    "s_waitcnt lgkmcnt(2)\n\t"
    "v_add_f32 v37, v32, v34\n\t"
    "v_add_f32 v38, v29, v31\n\t"
    "v_add_f32 v39, v33, v35\n\t"
    "v_mfma_f32_32x32x16_bf16 v[56:71], v[220:223], v[108:111], v[56:71]\n\t"
    "s_add_u32 m0, s83, 0xcc00\n\t"
    "s_add_u32 s82, s73, 0\n\t"
    "buffer_load_dwordx4 v16, s[36:39], s82 offen nt lds\n\t"
    "ds_read_b128 v[104:107], v6 offset:10064\n\t"
    "v_add_f32 v36, v36, v37\n\t"
    "v_add_f32 v37, v38, v39\n\t"
    "v_mul_f32 v36, 0x3e800000, v36\n\t"
    "v_sub_f32 v28, v28, v36\n\t"
    "v_mfma_f32_32x32x16_bf16 v[72:87], v[192:195], v[88:91], v[72:87]\n\t"
    "ds_read_b128 v[108:111], v6 offset:10128\n\t"
    "v_sub_f32 v30, v30, v36\n\t"
    "v_sub_f32 v32, v32, v36\n\t"
    "v_sub_f32 v34, v34, v36\n\t"
    "v_mul_f32 v38, v28, v28\n\t"
    "v_mfma_f32_32x32x16_bf16 v[72:87], v[196:199], v[92:95], v[72:87]\n\t"
    "ds_read_b128 v[88:91], v6 offset:10192\n\t"
    "v_mul_f32 v39, v30, v30\n\t"
    "v_fmac_f32 v38, v32, v32\n\t"
    "s_add_u32 m0, s83, 0xec80\n\t"
    "s_add_u32 s82, s73, 0x10000\n\t"
    "buffer_load_dwordx4 v16, s[36:39], s82 offen nt lds\n\t"
    "v_fmac_f32 v39, v34, v34\n\t"
    "v_add_f32 v38, v38, v39\n\t"
    "s_waitcnt lgkmcnt(3)\n\t"
    "v_mfma_f32_32x32x16_bf16 v[72:87], v[200:203], v[96:99], v[72:87]\n\t"
    "ds_read_b128 v[92:95], v6 offset:10256\n\t"
    "v_fmamk_f32 v37, v38, 0x42000000, v37\n\t"
    "v_sub_f32 v38, v26, v36\n\t"
    "s_waitcnt vmcnt(4)\n\t"
    "ds_read_b64_tr_b16 v[244:245], v14\n\t"
    "ds_read_b64_tr_b16 v[246:247], v14 offset:256\n\t"
    "v_mul_f32 v37, 0x3c000000, v37\n\t"
    "v_add_f32 v37, 0x3727c5ac, v37\n\t"
    "v_mfma_f32_32x32x16_bf16 v[72:87], v[204:207], v[100:103], v[72:87]\n\t"
    "ds_read_b128 v[96:99], v6 offset:10336\n\t"
    "v_rsq_f32 v37, v37\n\t"
    "s_nop 0\n\t"
    "v_mul_f32 v21, v37, v20\n\t"
    "v_mul_f32 v22, v38, v21\n\t"
    "s_add_u32 s82, s22, 0\n\t"
    "s_add_u32 m0, s82, 0x18f00\n\t"
    "s_nop 0\n\t"
    "buffer_load_dwordx4 v24, s[52:55], s82 offen lds\n\t"
    "s_waitcnt lgkmcnt(5)\n\t"
    "v_mfma_f32_32x32x16_bf16 v[56:71], v[224:227], v[104:107], v[56:71]\n\t"
    "ds_read_b128 v[100:103], v6 offset:10400\n\t"
    "v_fma_f32 v40, v40, v21, v22\n\t"
    "v_fma_f32 v41, v41, v21, v22\n\t"
    "v_fma_f32 v42, v42, v21, v22\n\t"
    "v_fma_f32 v43, v43, v21, v22\n\t"
    "v_mfma_f32_32x32x16_bf16 v[56:71], v[228:231], v[108:111], v[56:71]\n\t"
    "ds_read_b128 v[104:107], v6 offset:10464\n\t"
    "v_fma_f32 v44, v44, v21, v22\n\t"
    "v_fma_f32 v45, v45, v21, v22\n\t"
    "s_waitcnt vmcnt(4)\n\t"
    "ds_read_b64_tr_b16 v[248:249], v14 offset:1024\n\t"
    "ds_read_b64_tr_b16 v[250:251], v14 offset:1280\n\t"
    "v_fma_f32 v46, v46, v21, v22\n\t"
    "v_fma_f32 v47, v47, v21, v22\n\t"
    "s_waitcnt lgkmcnt(7)\n\t"
    "s_add_u32 s82, s22, 0x400\n\t"
    "s_add_u32 m0, s82, 0x18f00\n\t"
    "s_nop 0\n\t"
    "buffer_load_dwordx4 v24, s[52:55], s82 offen lds\n\t"
    "v_mfma_f32_32x32x16_bf16 v[56:71], v[232:235], v[88:91], v[56:71]\n\t"
    "ds_read_b128 v[108:111], v6 offset:10528\n\t"
    "v_mul_f32 v32, 0xbfb8aa3b, v40\n\t"
    "v_mul_f32 v33, 0xbfb8aa3b, v41\n\t"
    "v_mul_f32 v34, 0xbfb8aa3b, v42\n\t"
    "v_mul_f32 v35, 0xbfb8aa3b, v43\n\t"
    "v_mfma_f32_32x32x16_bf16 v[56:71], v[236:239], v[92:95], v[56:71]\n\t"
    "s_waitcnt vmcnt(4)\n\t"
    "ds_read_b64_tr_b16 v[252:253], v14 offset:2048\n\t"
    "ds_read_b64_tr_b16 v[254:255], v14 offset:2304\n\t"
    "ds_read_b128 v[88:91], v6 offset:19584\n\t"
    "v_mul_f32 v36, 0xbfb8aa3b, v44\n\t"
    "v_mul_f32 v37, 0xbfb8aa3b, v45\n\t"
    "v_mul_f32 v38, 0xbfb8aa3b, v46\n\t"
    "v_mul_f32 v39, 0xbfb8aa3b, v47\n\t"
    "s_waitcnt lgkmcnt(7)\n\t"
    "v_mfma_f32_32x32x16_bf16 v[56:71], v[240:243], v[96:99], v[56:71]\n\t"
    "ds_read_b128 v[92:95], v6 offset:19648\n\t"
    "s_add_u32 s82, s22, 0x800\n\t"
    "s_add_u32 m0, s82, 0x18f00\n\t"
    "s_nop 0\n\t"
    "buffer_load_dwordx4 v24, s[52:55], s82 offen lds\n\t"
    "v_exp_f32 v32, v32\n\t"
    "v_exp_f32 v33, v33\n\t"
    "v_mfma_f32_32x32x16_bf16 v[56:71], v[244:247], v[100:103], v[56:71]\n\t"
    "ds_read_b128 v[96:99], v6 offset:19712\n\t"
    "v_exp_f32 v34, v34\n\t"
    "v_exp_f32 v35, v35\n\t"
    "s_waitcnt lgkmcnt(5)\n\t"
    "v_mfma_f32_32x32x16_bf16 v[56:71], v[248:251], v[104:107], v[56:71]\n\t"
    "ds_read_b128 v[100:103], v6 offset:19776\n\t"
    "v_exp_f32 v36, v36\n\t"
    "s_add_u32 s82, s22, 0xc00\n\t"
    "s_add_u32 m0, s82, 0x18f00\n\t"
    "s_nop 0\n\t"
    "buffer_load_dwordx4 v24, s[52:55], s82 offen lds\n\t"
    "v_exp_f32 v37, v37\n\t"
    "s_waitcnt lgkmcnt(4)\n\t"
    "v_mfma_f32_32x32x16_bf16 v[56:71], v[252:255], v[108:111], v[56:71]\n\t"
    "ds_read_b128 v[104:107], v6 offset:19856\n\t"
    "v_exp_f32 v38, v38\n\t"
    "v_exp_f32 v39, v39\n\t"
    "s_waitcnt lgkmcnt(3)\n\t"
    "v_mfma_f32_32x32x16_bf16 v[72:87], v[208:211], v[88:91], v[72:87]\n\t"
    "ds_read_b128 v[108:111], v6 offset:19920\n\t"
    "v_add_f32 v32, 1.0, v32\n\t"
    "v_add_f32 v33, 1.0, v33\n\t"
    "v_add_f32 v34, 1.0, v34\n\t"
    "v_add_f32 v35, 1.0, v35\n\t"
    "v_mfma_f32_32x32x16_bf16 v[72:87], v[212:215], v[92:95], v[72:87]\n\t"
    "ds_read_b128 v[88:91], v6 offset:19984\n\t"
    "ds_write_b128 v9, v[64:67]\n\t"
    "ds_write_b128 v9, v[68:71] offset:1024\n\t"
    "v_add_f32 v36, 1.0, v36\n\t"
    "v_add_f32 v37, 1.0, v37\n\t"
    "v_add_f32 v38, 1.0, v38\n\t"
    "v_add_f32 v39, 1.0, v39\n\t"
    "s_waitcnt lgkmcnt(5)\n\t"
    "v_mfma_f32_32x32x16_bf16 v[72:87], v[216:219], v[96:99], v[72:87]\n\t"
    "ds_read_b128 v[92:95], v6 offset:20048\n\t"
    "s_waitcnt lgkmcnt(1)\n\t"
    "s_barrier\n\t"
    "v_rcp_f32 v32, v32\n\t"
    "v_rcp_f32 v33, v33\n\t"
    "v_mfma_f32_32x32x16_bf16 v[72:87], v[220:223], v[100:103], v[72:87]\n\t"
    "ds_read_b128 v[96:99], v6 offset:20128\n\t"
    "ds_read_b128 v[64:67], v10\n\t"
    "ds_read_b128 v[68:71], v10 offset:1024\n\t"
    "v_rcp_f32 v34, v34\n\t"
    "v_rcp_f32 v35, v35\n\t"
    "v_mfma_f32_32x32x16_bf16 v[72:87], v[224:227], v[104:107], v[72:87]\n\t"
    "ds_read_b128 v[100:103], v6 offset:20192\n\t"
    "v_rcp_f32 v36, v36\n\t"
    "v_rcp_f32 v37, v37\n\t"
    "v_mfma_f32_32x32x16_bf16 v[72:87], v[228:231], v[108:111], v[72:87]\n\t"
    "ds_read_b128 v[104:107], v6 offset:20256\n\t"
    "s_waitcnt lgkmcnt(3)\n\t"
    "v_add_f32 v56, v56, v64\n\t"
    "v_add_f32 v57, v57, v65\n\t"
    "v_add_f32 v58, v58, v66\n\t"
    "v_add_f32 v59, v59, v67\n\t"
    "s_waitcnt lgkmcnt(2)\n\t"
    "v_add_f32 v60, v60, v68\n\t"
    "v_add_f32 v61, v61, v69\n\t"
    "v_add_f32 v62, v62, v70\n\t"
    "v_add_f32 v63, v63, v71\n\t"
    "v_cvt_pk_bf16_f32 v64, v56, v57\n\t"
    "v_cvt_pk_bf16_f32 v65, v58, v59\n\t"
    "v_cvt_pk_bf16_f32 v66, v60, v61\n\t"
    "v_cvt_pk_bf16_f32 v67, v62, v63\n\t"
    "ds_write_b128 v5, v[64:67]\n\t"
    "v_rcp_f32 v38, v38\n\t"
    "v_rcp_f32 v39, v39\n\t"
    "v_mfma_f32_32x32x16_bf16 v[72:87], v[232:235], v[88:91], v[72:87]\n\t"
    "ds_read_b128 v[108:111], v6 offset:20320\n\t"
    "ds_read_b128 v[60:63], v8 offset:22880\n\t"
    "v_mul_f32 v40, v40, v32\n\t"
    "v_mul_f32 v41, v41, v33\n\t"
    "v_mul_f32 v42, v42, v34\n\t"
    "v_mul_f32 v43, v43, v35\n\t"
    "v_mfma_f32_32x32x16_bf16 v[72:87], v[236:239], v[92:95], v[72:87]\n\t"
    "ds_read_b128 v[88:91], v6 offset:19584\n\t"
    "v_mul_f32 v44, v44, v36\n\t"
    "v_mul_f32 v45, v45, v37\n\t"
    "v_mul_f32 v46, v46, v38\n\t"
    "v_mul_f32 v47, v47, v39\n\t"
    "s_waitcnt lgkmcnt(5)\n\t"
    "v_mfma_f32_32x32x16_bf16 v[72:87], v[240:243], v[96:99], v[72:87]\n\t"
    "ds_read_b128 v[92:95], v6 offset:19648\n\t"
    "s_waitcnt lgkmcnt(2)\n\t"
    "v_lshlrev_b32 v56, 16, v60\n\t"
    "v_and_b32 v57, 0xffff0000, v60\n\t"
    "v_lshlrev_b32 v58, 16, v61\n\t"
    "v_and_b32 v59, 0xffff0000, v61\n\t"
    "v_lshlrev_b32 v60, 16, v62\n\t"
    "v_and_b32 v61, 0xffff0000, v62\n\t"
    "v_lshlrev_b32 v62, 16, v63\n\t"
    "v_and_b32 v63, 0xffff0000, v63\n\t"
    "v_mov_b32 v64, 0\n\t"
    "v_mov_b32 v65, 0\n\t"
    "v_mov_b32 v66, 0\n\t"
    "v_mov_b32 v67, 0\n\t"
    "v_mov_b32 v68, 0\n\t"
    "v_mov_b32 v69, 0\n\t"
    "v_mov_b32 v70, 0\n\t"
    "v_mov_b32 v71, 0\n\t"
    "v_cvt_pk_bf16_f32 v28, v40, v41\n\t"
    "v_cvt_pk_bf16_f32 v29, v42, v43\n\t"
    "v_cvt_pk_bf16_f32 v30, v44, v45\n\t"
    "v_cvt_pk_bf16_f32 v31, v46, v47\n\t"
    "v_mfma_f32_32x32x16_bf16 v[72:87], v[244:247], v[100:103], v[72:87]\n\t"
    "ds_read_b128 v[96:99], v6 offset:19712\n\t"
    "s_barrier\n\t"
    "ds_write_b128 v3, v[28:31] offset:34816\n\t"
    "v_fma_f32 v48, v48, v21, v22\n\t"
    "v_fma_f32 v49, v49, v21, v22\n\t"
    "v_fma_f32 v50, v50, v21, v22\n\t"
    "v_mfma_f32_32x32x16_bf16 v[72:87], v[248:251], v[104:107], v[72:87]\n\t"
    "ds_read_b128 v[100:103], v6 offset:19776\n\t"
    "v_fma_f32 v51, v51, v21, v22\n\t"
    "v_fma_f32 v52, v52, v21, v22\n\t"
    "v_fma_f32 v53, v53, v21, v22\n\t"
    "v_fma_f32 v54, v54, v21, v22\n\t"
    "v_mfma_f32_32x32x16_bf16 v[72:87], v[252:255], v[108:111], v[72:87]\n\t"
    "ds_read_b128 v[104:107], v6 offset:19856\n\t"
    "v_fma_f32 v55, v55, v21, v22\n\t"
    "v_mul_f32 v32, 0xbfb8aa3b, v48\n\t"
    "v_mul_f32 v33, 0xbfb8aa3b, v49\n\t"
    "v_mul_f32 v34, 0xbfb8aa3b, v50\n\t"
    "s_waitcnt lgkmcnt(4)\n\t"
    "v_mfma_f32_32x32x16_bf16 v[56:71], v[112:115], v[88:91], v[56:71]\n\t"
    "ds_read_b128 v[108:111], v6 offset:19920\n\t"
    "v_mul_f32 v35, 0xbfb8aa3b, v51\n\t"
    "v_mul_f32 v36, 0xbfb8aa3b, v52\n\t"
    "v_mul_f32 v37, 0xbfb8aa3b, v53\n\t"
    "v_mul_f32 v38, 0xbfb8aa3b, v54\n\t"
    "v_mfma_f32_32x32x16_bf16 v[56:71], v[116:119], v[92:95], v[56:71]\n\t"
    "ds_read_b128 v[88:91], v6 offset:19984\n\t"
    "v_mul_f32 v39, 0xbfb8aa3b, v55\n\t"
    "v_exp_f32 v32, v32\n\t"
    "s_waitcnt lgkmcnt(3)\n\t"
    "v_mfma_f32_32x32x16_bf16 v[56:71], v[120:123], v[96:99], v[56:71]\n\t"
    "ds_read_b128 v[92:95], v6 offset:20048\n\t"
    "v_exp_f32 v33, v33\n\t"
    "v_exp_f32 v34, v34\n\t"
    "ds_write_b128 v9, v[80:83]\n\t"
    "ds_write_b128 v9, v[84:87] offset:1024\n\t"
    "v_mfma_f32_32x32x16_bf16 v[56:71], v[124:127], v[100:103], v[56:71]\n\t"
    "ds_read_b128 v[96:99], v6 offset:20128\n\t"
    "v_exp_f32 v35, v35\n\t"
    "v_exp_f32 v36, v36\n\t"
    "s_waitcnt lgkmcnt(5)\n\t"
    "v_mfma_f32_32x32x16_bf16 v[56:71], v[128:131], v[104:107], v[56:71]\n\t"
    "ds_read_b128 v[100:103], v6 offset:20192\n\t"
    "v_exp_f32 v37, v37\n\t"
    "v_exp_f32 v38, v38\n\t"
    "v_mfma_f32_32x32x16_bf16 v[56:71], v[132:135], v[108:111], v[56:71]\n\t"
    "ds_read_b128 v[104:107], v6 offset:20256\n\t"
    "v_exp_f32 v39, v39\n\t"
    "v_add_f32 v32, 1.0, v32\n\t"
    "v_add_f32 v33, 1.0, v33\n\t"
    "s_waitcnt lgkmcnt(5)\n\t"
    "v_mfma_f32_32x32x16_bf16 v[56:71], v[136:139], v[88:91], v[56:71]\n\t"
    "ds_read_b128 v[108:111], v6 offset:20320\n\t"
    "v_add_f32 v34, 1.0, v34\n\t"
    "v_add_f32 v35, 1.0, v35\n\t"
    "v_add_f32 v36, 1.0, v36\n\t"
    "v_add_f32 v37, 1.0, v37\n\t"
    "v_mfma_f32_32x32x16_bf16 v[56:71], v[140:143], v[92:95], v[56:71]\n\t"
    "ds_read_b128 v[88:91], v6 offset:24480\n\t"
    "v_add_f32 v38, 1.0, v38\n\t"
    "v_add_f32 v39, 1.0, v39\n\t"
    "v_rcp_f32 v32, v32\n\t"
    "s_waitcnt lgkmcnt(3)\n\t"
    "v_mfma_f32_32x32x16_bf16 v[56:71], v[144:147], v[96:99], v[56:71]\n\t"
    "ds_read_b128 v[92:95], v6 offset:24544\n\t"
    "v_rcp_f32 v33, v33\n\t"
    "v_rcp_f32 v34, v34\n\t"
    "v_mfma_f32_32x32x16_bf16 v[56:71], v[148:151], v[100:103], v[56:71]\n\t"
    "ds_read_b128 v[96:99], v6 offset:24608\n\t"
    "v_rcp_f32 v35, v35\n\t"
    "v_rcp_f32 v36, v36\n\t"
    "s_waitcnt lgkmcnt(3)\n\t"
    "v_mfma_f32_32x32x16_bf16 v[56:71], v[152:155], v[104:107], v[56:71]\n\t"
    "ds_read_b128 v[100:103], v6 offset:24672\n\t"
    "v_rcp_f32 v37, v37\n\t"
    "v_rcp_f32 v38, v38\n\t"
    "v_mfma_f32_32x32x16_bf16 v[56:71], v[156:159], v[108:111], v[56:71]\n\t"
    "ds_read_b128 v[104:107], v6 offset:24752\n\t"
    "v_rcp_f32 v39, v39\n\t"
    "v_mul_f32 v48, v48, v32\n\t"
    "v_mul_f32 v49, v49, v33\n\t"
    "s_waitcnt lgkmcnt(3)\n\t"
    "v_mfma_f32_32x32x16_bf16 v[56:71], v[160:163], v[88:91], v[56:71]\n\t"
    "ds_read_b128 v[108:111], v6 offset:24816\n\t"
    "v_mul_f32 v50, v50, v34\n\t"
    "v_mul_f32 v51, v51, v35\n\t"
    "v_mul_f32 v52, v52, v36\n\t"
    "v_mul_f32 v53, v53, v37\n\t"
    "v_mfma_f32_32x32x16_bf16 v[56:71], v[164:167], v[92:95], v[56:71]\n\t"
    "ds_read_b128 v[88:91], v6 offset:24880\n\t"
    "v_mul_f32 v54, v54, v38\n\t"
    "v_mul_f32 v55, v55, v39\n\t"
    "v_cvt_pk_bf16_f32 v28, v48, v49\n\t"
    "v_cvt_pk_bf16_f32 v29, v50, v51\n\t"
    "s_waitcnt lgkmcnt(3)\n\t"
    "v_mfma_f32_32x32x16_bf16 v[56:71], v[168:171], v[96:99], v[56:71]\n\t"
    "ds_read_b128 v[92:95], v6 offset:24944\n\t"
    "v_cvt_pk_bf16_f32 v30, v52, v53\n\t"
    "v_cvt_pk_bf16_f32 v31, v54, v55\n\t"
    "ds_write_b128 v3, v[28:31] offset:34832\n\t"
    "v_mfma_f32_32x32x16_bf16 v[56:71], v[172:175], v[100:103], v[56:71]\n\t"
    "ds_read_b128 v[96:99], v6 offset:25024\n\t"
    "s_waitcnt lgkmcnt(4)\n\t"
    "v_mfma_f32_32x32x16_bf16 v[56:71], v[176:179], v[104:107], v[56:71]\n\t"
    "ds_read_b128 v[100:103], v6 offset:25088\n\t"
    "v_mfma_f32_32x32x16_bf16 v[56:71], v[180:183], v[108:111], v[56:71]\n\t"
    "ds_read_b128 v[104:107], v6 offset:25152\n\t"
    "s_waitcnt lgkmcnt(4)\n\t"
    "v_mfma_f32_32x32x16_bf16 v[56:71], v[184:187], v[88:91], v[56:71]\n\t"
    "ds_read_b128 v[108:111], v6 offset:25216\n\t"
    "s_waitcnt lgkmcnt(4)\n\t"
    "s_barrier\n\t"
    "v_mfma_f32_32x32x16_bf16 v[56:71], v[188:191], v[92:95], v[56:71]\n\t"
    "ds_read_b128 v[88:91], v6 offset:29376\n\t"
    "ds_read_b128 v[80:83], v10\n\t"
    "ds_read_b128 v[84:87], v10 offset:1024\n\t"
    "s_waitcnt lgkmcnt(5)\n\t"
    "v_mfma_f32_32x32x16_bf16 v[56:71], v[192:195], v[96:99], v[56:71]\n\t"
    "ds_read_b128 v[92:95], v6 offset:29440\n\t"
    "v_mfma_f32_32x32x16_bf16 v[56:71], v[196:199], v[100:103], v[56:71]\n\t"
    "ds_read_b128 v[96:99], v6 offset:29504\n\t"
    "s_waitcnt lgkmcnt(3)\n\t"
    "v_add_f32 v72, v72, v80\n\t"
    "v_add_f32 v73, v73, v81\n\t"
    "v_add_f32 v74, v74, v82\n\t"
    "v_add_f32 v75, v75, v83\n\t"
    "s_waitcnt lgkmcnt(2)\n\t"
    "v_add_f32 v76, v76, v84\n\t"
    "v_add_f32 v77, v77, v85\n\t"
    "v_add_f32 v78, v78, v86\n\t"
    "v_add_f32 v79, v79, v87\n\t"
    "v_cvt_pk_bf16_f32 v80, v72, v73\n\t"
    "v_cvt_pk_bf16_f32 v81, v74, v75\n\t"
    "v_cvt_pk_bf16_f32 v82, v76, v77\n\t"
    "v_cvt_pk_bf16_f32 v83, v78, v79\n\t"
    "ds_write_b128 v5, v[80:83] offset:8704\n\t"
    "v_mfma_f32_32x32x16_bf16 v[56:71], v[200:203], v[104:107], v[56:71]\n\t"
    "ds_read_b128 v[100:103], v6 offset:29568\n\t"
    "ds_read_b128 v[76:79], v8 offset:32240\n\t"
    "v_mfma_f32_32x32x16_bf16 v[56:71], v[204:207], v[108:111], v[56:71]\n\t"
    "ds_read_b128 v[104:107], v6 offset:29648\n\t"
    "s_waitcnt lgkmcnt(5)\n\t"
    "v_mfma_f32_32x32x16_bf16 v[56:71], v[208:211], v[88:91], v[56:71]\n\t"
    "ds_read_b128 v[108:111], v6 offset:29712\n\t"
    "v_mfma_f32_32x32x16_bf16 v[56:71], v[212:215], v[92:95], v[56:71]\n\t"
    "ds_read_b128 v[88:91], v6 offset:29776\n\t"
    "s_waitcnt lgkmcnt(4)\n\t"
    "v_mfma_f32_32x32x16_bf16 v[56:71], v[216:219], v[96:99], v[56:71]\n\t"
    "ds_read_b128 v[92:95], v6 offset:29840\n\t"
    "v_mfma_f32_32x32x16_bf16 v[56:71], v[220:223], v[100:103], v[56:71]\n\t"
    "ds_read_b128 v[96:99], v6 offset:29920\n\t"
    "s_waitcnt lgkmcnt(3)\n\t"
    "v_mfma_f32_32x32x16_bf16 v[56:71], v[224:227], v[104:107], v[56:71]\n\t"
    "ds_read_b128 v[100:103], v6 offset:29984\n\t"
    "v_mfma_f32_32x32x16_bf16 v[56:71], v[228:231], v[108:111], v[56:71]\n\t"
    "ds_read_b128 v[104:107], v6 offset:30048\n\t"
    "s_waitcnt lgkmcnt(3)\n\t"
    "v_mfma_f32_32x32x16_bf16 v[56:71], v[232:235], v[88:91], v[56:71]\n\t"
    "ds_read_b128 v[108:111], v6 offset:30112\n\t"
    "s_waitcnt vmcnt(0)\n\t"
    "s_barrier\n\t"
    "s_add_u32 m0, s83, 0x10d00\n\t"
    "s_add_u32 s82, s73, 0x20000\n\t"
    "buffer_load_dwordx4 v16, s[36:39], s82 offen nt lds\n\t"
    "v_mfma_f32_32x32x16_bf16 v[56:71], v[236:239], v[92:95], v[56:71]\n\t"
    "ds_read_b128 v[88:91], v7\n\t"
    "v_lshlrev_b32 v72, 16, v76\n\t"
    "v_and_b32 v73, 0xffff0000, v76\n\t"
    "v_lshlrev_b32 v74, 16, v77\n\t"
    "v_and_b32 v75, 0xffff0000, v77\n\t"
    "v_lshlrev_b32 v76, 16, v78\n\t"
    "v_and_b32 v77, 0xffff0000, v78\n\t"
    "v_lshlrev_b32 v78, 16, v79\n\t"
    "v_and_b32 v79, 0xffff0000, v79\n\t"
    "v_mov_b32 v80, 0\n\t"
    "v_mov_b32 v81, 0\n\t"
    "v_mov_b32 v82, 0\n\t"
    "v_mov_b32 v83, 0\n\t"
    "v_mov_b32 v84, 0\n\t"
    "v_mov_b32 v85, 0\n\t"
    "v_mov_b32 v86, 0\n\t"
    "v_mov_b32 v87, 0\n\t"
    "ds_read_b128 v[44:47], v4\n\t"
    "ds_read_b128 v[52:55], v4 offset:16\n\t"
    "s_waitcnt lgkmcnt(5)\n\t"
    "v_mfma_f32_32x32x16_bf16 v[56:71], v[240:243], v[96:99], v[56:71]\n\t"
    "ds_read_b128 v[92:95], v7 offset:32\n\t"
    "v_mfma_f32_32x32x16_bf16 v[56:71], v[244:247], v[100:103], v[56:71]\n\t"
    "ds_read_b128 v[96:99], v7 offset:64\n\t"
    "s_waitcnt lgkmcnt(3)\n\t"
    "v_lshlrev_b32 v40, 16, v44\n\t"
    "v_and_b32 v41, 0xffff0000, v44\n\t"
    "v_lshlrev_b32 v42, 16, v45\n\t"
    "v_and_b32 v43, 0xffff0000, v45\n\t"
    "v_lshlrev_b32 v44, 16, v46\n\t"
    "v_and_b32 v45, 0xffff0000, v46\n\t"
    "v_lshlrev_b32 v46, 16, v47\n\t"
    "v_and_b32 v47, 0xffff0000, v47\n\t"
    "s_waitcnt lgkmcnt(2)\n\t"
    "v_lshlrev_b32 v48, 16, v52\n\t"
    "v_and_b32 v49, 0xffff0000, v52\n\t"
    "v_lshlrev_b32 v50, 16, v53\n\t"
    "v_and_b32 v51, 0xffff0000, v53\n\t"
    "v_lshlrev_b32 v52, 16, v54\n\t"
    "v_and_b32 v53, 0xffff0000, v54\n\t"
    "v_lshlrev_b32 v54, 16, v55\n\t"
    "v_and_b32 v55, 0xffff0000, v55\n\t"
    "v_mfma_f32_32x32x16_bf16 v[56:71], v[248:251], v[104:107], v[56:71]\n\t"
    "ds_read_b128 v[100:103], v7 offset:96\n\t"
    "ds_read_b64_tr_b16 v[28:29], v13\n\t"
    "ds_read_b64_tr_b16 v[30:31], v13 offset:1024\n\t"
    "v_mfma_f32_32x32x16_bf16 v[56:71], v[252:255], v[108:111], v[56:71]\n\t"
    "ds_read_b128 v[104:107], v7 offset:128\n\t"
    "ds_read_b64_tr_b16 v[32:33], v13 offset:4096\n\t"
    "ds_read_b64_tr_b16 v[34:35], v13 offset:5120\n\t"
    "s_waitcnt lgkmcnt(7)\n\t"
    "s_waitcnt lgkmcnt(3)\n\t"
    "v_mfma_f32_32x32x16_bf16 v[40:55], v[28:31], v[88:91], v[40:55]\n\t"
    "ds_read_b128 v[108:111], v7 offset:160\n\t"
    "ds_read_b64_tr_b16 v[28:29], v13 offset:8192\n\t"
    "ds_read_b64_tr_b16 v[30:31], v13 offset:9216\n\t"
    "s_waitcnt lgkmcnt(3)\n\t"
    "v_mfma_f32_32x32x16_bf16 v[40:55], v[32:35], v[92:95], v[40:55]\n\t"
    "ds_read_b128 v[88:91], v7 offset:192\n\t"
    "ds_read_b64_tr_b16 v[32:33], v13 offset:12288\n\t"
    "ds_read_b64_tr_b16 v[34:35], v13 offset:13312\n\t"
    "ds_write_b128 v9, v[64:67]\n\t"
    "ds_write_b128 v9, v[68:71] offset:1024\n\t"
    "s_waitcnt lgkmcnt(5)\n\t"
    "v_mfma_f32_32x32x16_bf16 v[40:55], v[28:31], v[96:99], v[40:55]\n\t"
    "ds_read_b128 v[92:95], v7 offset:224\n\t"
    "ds_read_b64_tr_b16 v[28:29], v13 offset:16384\n\t"
    "ds_read_b64_tr_b16 v[30:31], v13 offset:17408\n\t"
    "s_waitcnt lgkmcnt(3)\n\t"
    "s_barrier\n\t"
    "s_add_u32 m0, s83, 0x12d80\n\t"
    "s_add_u32 s82, s73, 0x30000\n\t"
    "buffer_load_dwordx4 v16, s[36:39], s82 offen nt lds\n\t"
    "v_mfma_f32_32x32x16_bf16 v[40:55], v[32:35], v[100:103], v[40:55]\n\t"
    "ds_read_b128 v[96:99], v6 offset:29376\n\t"
    "ds_read_b64_tr_b16 v[32:33], v13 offset:20480\n\t"
    "ds_read_b64_tr_b16 v[34:35], v13 offset:21504\n\t"
    "ds_read_b128 v[64:67], v10\n\t"
    "ds_read_b128 v[68:71], v10 offset:1024\n\t"
    "s_waitcnt lgkmcnt(5)\n\t"
    "v_mfma_f32_32x32x16_bf16 v[40:55], v[28:31], v[104:107], v[40:55]\n\t"
    "ds_read_b128 v[100:103], v6 offset:29440\n\t"
    "ds_read_b64_tr_b16 v[28:29], v13 offset:24576\n\t"
    "ds_read_b64_tr_b16 v[30:31], v13 offset:25600\n\t"
    "s_waitcnt lgkmcnt(5)\n\t"
    "v_mfma_f32_32x32x16_bf16 v[40:55], v[32:35], v[108:111], v[40:55]\n\t"
    "ds_read_b128 v[104:107], v6 offset:29504\n\t"
    "ds_read_b64_tr_b16 v[32:33], v13 offset:28672\n\t"
    "ds_read_b64_tr_b16 v[34:35], v13 offset:29696\n\t"
    "s_waitcnt lgkmcnt(7)\n\t"
    "v_add_f32 v56, v56, v64\n\t"
    "v_add_f32 v57, v57, v65\n\t"
    "v_add_f32 v58, v58, v66\n\t"
    "v_add_f32 v59, v59, v67\n\t"
    "s_waitcnt lgkmcnt(6)\n\t"
    "v_add_f32 v60, v60, v68\n\t"
    "v_add_f32 v61, v61, v69\n\t"
    "v_add_f32 v62, v62, v70\n\t"
    "v_add_f32 v63, v63, v71\n\t"
    "v_cvt_pk_bf16_f32 v64, v56, v57\n\t"
    "v_cvt_pk_bf16_f32 v65, v58, v59\n\t"
    "v_cvt_pk_bf16_f32 v66, v60, v61\n\t"
    "v_cvt_pk_bf16_f32 v67, v62, v63\n\t"
    "ds_write_b128 v5, v[64:67] offset:17408\n\t"
    "s_waitcnt lgkmcnt(4)\n\t"
    "v_mfma_f32_32x32x16_bf16 v[40:55], v[28:31], v[88:91], v[40:55]\n\t"
    "ds_read_b128 v[108:111], v6 offset:29568\n\t"
    "s_waitcnt lgkmcnt(2)\n\t"
    "v_mfma_f32_32x32x16_bf16 v[40:55], v[32:35], v[92:95], v[40:55]\n\t"
    "ds_read_b128 v[88:91], v6 offset:29648\n\t"
    "v_mfma_f32_32x32x16_bf16 v[72:87], v[112:115], v[96:99], v[72:87]\n\t"
    "ds_read_b128 v[92:95], v6 offset:29712\n\t"
    "v_mfma_f32_32x32x16_bf16 v[72:87], v[116:119], v[100:103], v[72:87]\n\t"
    "ds_read_b128 v[96:99], v6 offset:29776\n\t"
    "s_waitcnt lgkmcnt(3)\n\t"
    "v_mfma_f32_32x32x16_bf16 v[72:87], v[120:123], v[104:107], v[72:87]\n\t"
    "ds_read_b128 v[100:103], v6 offset:29840\n\t"
    "s_nop 3\n\t"
    "v_cvt_pk_bf16_f32 v28, v40, v41\n\t"
    "v_cvt_pk_bf16_f32 v29, v42, v43\n\t"
    "v_cvt_pk_bf16_f32 v30, v44, v45\n\t"
    "v_mfma_f32_32x32x16_bf16 v[72:87], v[124:127], v[108:111], v[72:87]\n\t"
    "ds_read_b128 v[104:107], v6 offset:29920\n\t"
    "v_cvt_pk_bf16_f32 v31, v46, v47\n\t"
    "ds_write_b128 v4, v[28:31]\n\t"
    "v_cvt_pk_bf16_f32 v28, v48, v49\n\t"
    "s_waitcnt lgkmcnt(4)\n\t"
    "v_mfma_f32_32x32x16_bf16 v[72:87], v[128:131], v[88:91], v[72:87]\n\t"
    "ds_read_b128 v[108:111], v6 offset:29984\n\t"
    "v_cvt_pk_bf16_f32 v29, v50, v51\n\t"
    "v_cvt_pk_bf16_f32 v30, v52, v53\n\t"
    "v_cvt_pk_bf16_f32 v31, v54, v55\n\t"
    "v_mfma_f32_32x32x16_bf16 v[72:87], v[132:135], v[92:95], v[72:87]\n\t"
    "ds_read_b128 v[88:91], v6 offset:30048\n\t"
    "ds_write_b128 v4, v[28:31] offset:16\n\t"
    "s_waitcnt lgkmcnt(5)\n\t"
    "v_mfma_f32_32x32x16_bf16 v[72:87], v[136:139], v[96:99], v[72:87]\n\t"
    "ds_read_b128 v[92:95], v6 offset:30112\n\t"
    "v_mfma_f32_32x32x16_bf16 v[72:87], v[140:143], v[100:103], v[72:87]\n\t"
    "ds_read_b128 v[96:99], v6 offset:34272\n\t"
    "s_waitcnt lgkmcnt(2)\n\t"
    "s_barrier\n\t"
    "v_mfma_f32_32x32x16_bf16 v[72:87], v[144:147], v[104:107], v[72:87]\n\t"
    "ds_read_b128 v[100:103], v6 offset:34336\n\t"
    "ds_read_b128 v[40:43], v17\n\t"
    "ds_read_b128 v[44:47], v17 offset:8320\n\t"
    "v_mfma_f32_32x32x16_bf16 v[72:87], v[148:151], v[108:111], v[72:87]\n\t"
    "ds_read_b128 v[104:107], v6 offset:34400\n\t"
    "s_waitcnt lgkmcnt(5)\n\t"
    "v_mfma_f32_32x32x16_bf16 v[72:87], v[152:155], v[88:91], v[72:87]\n\t"
    "ds_read_b128 v[108:111], v6 offset:34464\n\t"
    "v_mfma_f32_32x32x16_bf16 v[72:87], v[156:159], v[92:95], v[72:87]\n\t"
    "ds_read_b128 v[88:91], v6 offset:34544\n\t"
    "s_add_u32 s82, s73, 0\n\t"
    "s_waitcnt lgkmcnt(4)\n\t"
    "buffer_store_dwordx4 v[40:43], v16, s[60:63], s82 offen sc1\n\t"
    "v_mfma_f32_32x32x16_bf16 v[72:87], v[160:163], v[96:99], v[72:87]\n\t"
    "ds_read_b128 v[92:95], v6 offset:34608\n\t"
    "v_mfma_f32_32x32x16_bf16 v[72:87], v[164:167], v[100:103], v[72:87]\n\t"
    "ds_read_b128 v[96:99], v6 offset:34672\n\t"
    "s_add_u32 s82, s73, 0x10000\n\t"
    "s_waitcnt lgkmcnt(5)\n\t"
    "buffer_store_dwordx4 v[44:47], v16, s[60:63], s82 offen sc1\n\t"
    "s_waitcnt lgkmcnt(3)\n\t"
    "v_mfma_f32_32x32x16_bf16 v[72:87], v[168:171], v[104:107], v[72:87]\n\t"
    "ds_read_b128 v[100:103], v6 offset:34736\n\t"
    "v_mfma_f32_32x32x16_bf16 v[72:87], v[172:175], v[108:111], v[72:87]\n\t"
    "ds_read_b128 v[104:107], v6 offset:34816\n\t"
    "s_waitcnt lgkmcnt(3)\n\t"
    "v_mfma_f32_32x32x16_bf16 v[72:87], v[176:179], v[88:91], v[72:87]\n\t"
    "ds_read_b128 v[108:111], v6 offset:34880\n\t"
    "v_mfma_f32_32x32x16_bf16 v[72:87], v[180:183], v[92:95], v[72:87]\n\t"
    "ds_read_b128 v[88:91], v6 offset:34944\n\t"
    "s_waitcnt lgkmcnt(3)\n\t"
    "v_mfma_f32_32x32x16_bf16 v[72:87], v[184:187], v[96:99], v[72:87]\n\t"
    "ds_read_b128 v[92:95], v6 offset:35008\n\t"
    "v_mfma_f32_32x32x16_bf16 v[72:87], v[188:191], v[100:103], v[72:87]\n\t"
    "ds_read_b128 v[96:99], v6 offset:39168\n\t"
    "s_waitcnt lgkmcnt(3)\n\t"
    "v_mfma_f32_32x32x16_bf16 v[72:87], v[192:195], v[104:107], v[72:87]\n\t"
    "ds_read_b128 v[100:103], v6 offset:39232\n\t"
    "v_mfma_f32_32x32x16_bf16 v[72:87], v[196:199], v[108:111], v[72:87]\n\t"
    "ds_read_b128 v[104:107], v6 offset:39296\n\t"
    "s_waitcnt lgkmcnt(3)\n\t"
    "v_mfma_f32_32x32x16_bf16 v[72:87], v[200:203], v[88:91], v[72:87]\n\t"
    "ds_read_b128 v[108:111], v6 offset:39360\n\t"
    "v_mfma_f32_32x32x16_bf16 v[72:87], v[204:207], v[92:95], v[72:87]\n\t"
    "ds_read_b128 v[88:91], v6 offset:39440\n\t"
    "s_waitcnt lgkmcnt(3)\n\t"
    "v_mfma_f32_32x32x16_bf16 v[72:87], v[208:211], v[96:99], v[72:87]\n\t"
    "ds_read_b128 v[92:95], v6 offset:39504\n\t"
    "v_mfma_f32_32x32x16_bf16 v[72:87], v[212:215], v[100:103], v[72:87]\n\t"
    "ds_read_b128 v[96:99], v6 offset:39568\n\t"
    "s_waitcnt lgkmcnt(3)\n\t"
    "v_mfma_f32_32x32x16_bf16 v[72:87], v[216:219], v[104:107], v[72:87]\n\t"
    "ds_read_b128 v[100:103], v6 offset:39632\n\t"
    "s_waitcnt vmcnt(2)\n\t"
    "s_barrier\n\t"
    "ds_read_b128 v[44:47], v4 offset:16640\n\t"
    "ds_read_b128 v[52:55], v4 offset:16656\n\t"
    "v_mfma_f32_32x32x16_bf16 v[72:87], v[220:223], v[108:111], v[72:87]\n\t"
    "ds_read_b128 v[104:107], v6 offset:39712\n\t"
    "s_waitcnt lgkmcnt(5)\n\t"
    "v_mfma_f32_32x32x16_bf16 v[72:87], v[224:227], v[88:91], v[72:87]\n\t"
    "ds_read_b128 v[108:111], v6 offset:39776\n\t"
    "v_mfma_f32_32x32x16_bf16 v[72:87], v[228:231], v[92:95], v[72:87]\n\t"
    "ds_read_b128 v[88:91], v6 offset:39840\n\t"
    "s_waitcnt lgkmcnt(4)\n\t"
    "v_lshlrev_b32 v40, 16, v44\n\t"
    "v_and_b32 v41, 0xffff0000, v44\n\t"
    "v_lshlrev_b32 v42, 16, v45\n\t"
    "v_and_b32 v43, 0xffff0000, v45\n\t"
    "v_lshlrev_b32 v44, 16, v46\n\t"
    "v_and_b32 v45, 0xffff0000, v46\n\t"
    "v_lshlrev_b32 v46, 16, v47\n\t"
    "v_and_b32 v47, 0xffff0000, v47\n\t"
    "s_waitcnt lgkmcnt(3)\n\t"
    "v_lshlrev_b32 v48, 16, v52\n\t"
    "v_and_b32 v49, 0xffff0000, v52\n\t"
    "v_lshlrev_b32 v50, 16, v53\n\t"
    "v_and_b32 v51, 0xffff0000, v53\n\t"
    "v_lshlrev_b32 v52, 16, v54\n\t"
    "v_and_b32 v53, 0xffff0000, v54\n\t"
    "v_lshlrev_b32 v54, 16, v55\n\t"
    "v_and_b32 v55, 0xffff0000, v55\n\t"
    "v_mfma_f32_32x32x16_bf16 v[72:87], v[232:235], v[96:99], v[72:87]\n\t"
    "ds_read_b128 v[92:95], v6 offset:39904\n\t"
    "v_mfma_f32_32x32x16_bf16 v[72:87], v[236:239], v[100:103], v[72:87]\n\t"
    "s_waitcnt lgkmcnt(2)\n\t"
    "v_mfma_f32_32x32x16_bf16 v[72:87], v[240:243], v[104:107], v[72:87]\n\t"
    "v_mfma_f32_32x32x16_bf16 v[72:87], v[244:247], v[108:111], v[72:87]\n\t"
    "s_waitcnt lgkmcnt(0)\n\t"
    "v_mfma_f32_32x32x16_bf16 v[72:87], v[248:251], v[88:91], v[72:87]\n\t"
    "v_mfma_f32_32x32x16_bf16 v[72:87], v[252:255], v[92:95], v[72:87]\n\t"
    "ds_read_b64_tr_b16 v[28:29], v13\n\t"
    "ds_read_b64_tr_b16 v[30:31], v13 offset:1024\n\t"
    "ds_read_b64_tr_b16 v[32:33], v13 offset:4096\n\t"
    "ds_read_b64_tr_b16 v[34:35], v13 offset:5120\n\t"
    "s_nop 7\n\t"
    "ds_write_b128 v9, v[80:83]\n\t"
    "ds_write_b128 v9, v[84:87] offset:1024\n\t"
    "s_waitcnt lgkmcnt(0)\n\t"
    "s_barrier\n\t"
    "ds_read_b128 v[80:83], v10\n\t"
    "ds_read_b128 v[84:87], v10 offset:1024\n\t"
    "s_waitcnt lgkmcnt(1)\n\t"
    "v_add_f32 v72, v72, v80\n\t"
    "v_add_f32 v73, v73, v81\n\t"
    "v_add_f32 v74, v74, v82\n\t"
    "v_add_f32 v75, v75, v83\n\t"
    "s_waitcnt lgkmcnt(0)\n\t"
    "v_add_f32 v76, v76, v84\n\t"
    "v_add_f32 v77, v77, v85\n\t"
    "v_add_f32 v78, v78, v86\n\t"
    "v_add_f32 v79, v79, v87\n\t"
    "v_cvt_pk_bf16_f32 v80, v72, v73\n\t"
    "v_cvt_pk_bf16_f32 v81, v74, v75\n\t"
    "v_cvt_pk_bf16_f32 v82, v76, v77\n\t"
    "v_cvt_pk_bf16_f32 v83, v78, v79\n\t"
    "ds_write_b128 v5, v[80:83] offset:26112\n\t"
    "s_waitcnt lgkmcnt(0)\n\t"
    "s_barrier\n\t"
    "ds_read_b128 v[96:99], v7 offset:17408\n\t"
    "ds_read_b128 v[100:103], v7 offset:17440\n\t"
    "ds_read_b128 v[104:107], v7 offset:17472\n\t"
    "ds_read_b128 v[108:111], v7 offset:17504\n\t"
    "ds_read_b128 v[88:91], v7 offset:17536\n\t"
    "s_waitcnt lgkmcnt(3)\n\t"
    "v_mfma_f32_32x32x16_bf16 v[40:55], v[28:31], v[96:99], v[40:55]\n\t"
    "ds_read_b128 v[92:95], v7 offset:17568\n\t"
    "ds_read_b64_tr_b16 v[28:29], v13 offset:8192\n\t"
    "ds_read_b64_tr_b16 v[30:31], v13 offset:9216\n\t"
    "v_mfma_f32_32x32x16_bf16 v[40:55], v[32:35], v[100:103], v[40:55]\n\t"
    "ds_read_b128 v[96:99], v7 offset:17600\n\t"
    "ds_read_b64_tr_b16 v[32:33], v13 offset:12288\n\t"
    "ds_read_b64_tr_b16 v[34:35], v13 offset:13312\n\t"
    "s_waitcnt lgkmcnt(7)\n\t"
    "s_waitcnt lgkmcnt(3)\n\t"
    "v_mfma_f32_32x32x16_bf16 v[40:55], v[28:31], v[104:107], v[40:55]\n\t"
    "ds_read_b128 v[100:103], v7 offset:17632\n\t"
    "ds_read_b64_tr_b16 v[28:29], v13 offset:16384\n\t"
    "ds_read_b64_tr_b16 v[30:31], v13 offset:17408\n\t"
    "s_waitcnt lgkmcnt(3)\n\t"
    "v_mfma_f32_32x32x16_bf16 v[40:55], v[32:35], v[108:111], v[40:55]\n\t"
    "ds_read_b64_tr_b16 v[32:33], v13 offset:20480\n\t"
    "ds_read_b64_tr_b16 v[34:35], v13 offset:21504\n\t"
    "s_waitcnt lgkmcnt(2)\n\t"
    "v_mfma_f32_32x32x16_bf16 v[40:55], v[28:31], v[88:91], v[40:55]\n\t"
    "ds_read_b64_tr_b16 v[28:29], v13 offset:24576\n\t"
    "ds_read_b64_tr_b16 v[30:31], v13 offset:25600\n\t"
    "s_waitcnt lgkmcnt(2)\n\t"
    "v_mfma_f32_32x32x16_bf16 v[40:55], v[32:35], v[92:95], v[40:55]\n\t"
    "ds_read_b64_tr_b16 v[32:33], v13 offset:28672\n\t"
    "ds_read_b64_tr_b16 v[34:35], v13 offset:29696\n\t"
    "s_waitcnt lgkmcnt(2)\n\t"
    "v_mfma_f32_32x32x16_bf16 v[40:55], v[28:31], v[96:99], v[40:55]\n\t"
    "s_waitcnt lgkmcnt(0)\n\t"
    "v_mfma_f32_32x32x16_bf16 v[40:55], v[32:35], v[100:103], v[40:55]\n\t"
    "s_nop 11\n\t"
    "v_cvt_pk_bf16_f32 v28, v40, v41\n\t"
    "v_cvt_pk_bf16_f32 v29, v42, v43\n\t"
    "v_cvt_pk_bf16_f32 v30, v44, v45\n\t"
    "v_cvt_pk_bf16_f32 v31, v46, v47\n\t"
    "ds_write_b128 v4, v[28:31] offset:16640\n\t"
    "v_cvt_pk_bf16_f32 v28, v48, v49\n\t"
    "v_cvt_pk_bf16_f32 v29, v50, v51\n\t"
    "v_cvt_pk_bf16_f32 v30, v52, v53\n\t"
    "v_cvt_pk_bf16_f32 v31, v54, v55\n\t"
    "ds_write_b128 v4, v[28:31] offset:16656\n\t"
    "s_waitcnt lgkmcnt(0)\n\t"
    "s_barrier\n\t"
    "ds_read_b128 v[40:43], v17 offset:16640\n\t"
    "ds_read_b128 v[44:47], v17 offset:24960\n\t"
    "s_add_u32 s82, s73, 0x20000\n\t"
    "s_waitcnt lgkmcnt(1)\n\t"
    "buffer_store_dwordx4 v[40:43], v16, s[60:63], s82 offen sc1\n\t"
    "s_add_u32 s82, s73, 0x30000\n\t"
    "s_waitcnt lgkmcnt(0)\n\t"
    "buffer_store_dwordx4 v[44:47], v16, s[60:63], s82 offen sc1\n\t"
    :
    : "s"(__builtin_amdgcn_kernarg_segment_ptr()), "s"(blockIdx.x), "v"(threadIdx.x)
    : "v1", "v2", "v3", "v4", "v5", "v6", "v7", "v8", "v9", "v10", "v11", "v12", "v13", "v14", "v15", "v16", "v17", "v18", "v19", "v20", "v21", "v22", "v23", "v24", "v25", "v26", "v27", "v28", "v29", "v30", "v31", "v32", "v33", "v34", "v35", "v36", "v37", "v38", "v39", "v40", "v41", "v42", "v43", "v44", "v45", "v46", "v47", "v48", "v49", "v50", "v51", "v52", "v53", "v54", "v55", "v56", "v57", "v58", "v59", "v60", "v61", "v62", "v63", "v64", "v65", "v66", "v67", "v68", "v69", "v70", "v71", "v72", "v73", "v74", "v75", "v76", "v77", "v78", "v79", "v80", "v81", "v82", "v83", "v84", "v85", "v86", "v87", "v88", "v89", "v90", "v91", "v92", "v93", "v94", "v95", "v96", "v97", "v98", "v99", "v100", "v101", "v102", "v103", "v104", "v105", "v106", "v107", "v108", "v109", "v110", "v111", "v112", "v113", "v114", "v115", "v116", "v117", "v118", "v119", "v120", "v121", "v122", "v123", "v124", "v125", "v126", "v127", "v128", "v129", "v130", "v131", "v132", "v133", "v134", "v135", "v136", "v137", "v138", "v139", "v140", "v141", "v142", "v143", "v144", "v145", "v146", "v147", "v148", "v149", "v150", "v151", "v152", "v153", "v154", "v155", "v156", "v157", "v158", "v159", "v160", "v161", "v162", "v163", "v164", "v165", "v166", "v167", "v168", "v169", "v170", "v171", "v172", "v173", "v174", "v175", "v176", "v177", "v178", "v179", "v180", "v181", "v182", "v183", "v184", "v185", "v186", "v187", "v188", "v189", "v190", "v191", "v192", "v193", "v194", "v195", "v196", "v197", "v198", "v199", "v200", "v201", "v202", "v203", "v204", "v205", "v206", "v207", "v208", "v209", "v210", "v211", "v212", "v213", "v214", "v215", "v216", "v217", "v218", "v219", "v220", "v221", "v222", "v223", "v224", "v225", "v226", "v227", "v228", "v229", "v230", "v231", "v232", "v233", "v234", "v235", "v236", "v237", "v238", "v239", "v240", "v241", "v242", "v243", "v244", "v245", "v246", "v247", "v248", "v249", "v250", "v251", "v252", "v253", "v254", "v255", "s4", "s5", "s6", "s7", "s8", "s9", "s10", "s11", "s12", "s13", "s14", "s15", "s16", "s17", "s18", "s19", "s20", "s21", "s22", "s23", "s24", "s25", "s26", "s27", "s28", "s29", "s30", "s31", "s36", "s37", "s38", "s39", "s40", "s41", "s42", "s43", "s44", "s45", "s46", "s47", "s48", "s49", "s50", "s51", "s52", "s53", "s54", "s55", "s56", "s57", "s58", "s59", "s60", "s61", "s62", "s63", "s64", "s65", "s66", "s67", "s68", "s69", "s70", "s71", "s72", "s73", "s74", "s75", "s76", "s77", "s78", "s79", "s80", "s81", "s82", "s83", "s84", "s85", "s86", "s87", "s88", "s89", "s90", "s91", "s92", "s93", "s94", "s95", "s96", "s97", "s98", "s99", "vcc", "scc", "memory");
}

extern "C" void kernel_launch(void* const* d_in, const int* in_sizes, int n_in,
                              void* d_out, int out_size, void* d_ws, size_t ws_size,
                              hipStream_t stream) {
  constexpr int SH = 163584;
  hipFuncSetAttribute((const void*)cn_kernel, hipFuncAttributeMaxDynamicSharedMemorySize, SH);
  cn_kernel<<<256, 512, SH, stream>>>(d_in[0], d_in[1], d_in[2], d_in[3], d_in[4], d_in[5], d_out, d_ws);
}
```
